```python
import torch

def _softmax(x, axis=-1):
    return torch.softmax(x, dim=axis)
def _gelu(x):
    return torch.nn.functional.gelu(x, approximate="tanh")
def _silu(x):
    return torch.nn.functional.silu(x)
def _sigmoid(x):
    return torch.sigmoid(x)
def _layernorm(x, axis=-1, eps=1e-5):
    m = x.mean(dim=axis, keepdim=True)
    v = x.var(dim=axis, keepdim=True, unbiased=False)
    return (x - m) / torch.sqrt(v + eps)
def _scatter_add(tgt, idx, src, axis=0):
    return tgt.clone().index_add_(axis, idx, src)
def _segment_sum(src, seg, n):
    out = torch.zeros((n,) + src.shape[1:], dtype=src.dtype)
    return out.index_add_(0, seg, src)
def _pad(x, pw):
    flat = tuple(v for pair in reversed(pw) for v in pair)
    return torch.nn.functional.pad(x, flat)

kernel_name = 'sequential_preLN__compressed_kv_cla__glu_gated_swiglu__decode'

def setup_inputs():

    _w = (lambda a: (lambda t: t.to(torch.bfloat16) if t.is_floating_point() else t)(torch.as_tensor(a)))
    def _A_setup():
        B, H, Sq, Sk, D, M = 2, 32, 1, 8192, 128, 640
        Dc = 4 * D
        return {
            "x": _w((lambda *s: torch.randn(*s, dtype=torch.bfloat16) * 0.05)(B, Sq, M)),
            "c_kv": _w((lambda *s: torch.randn(*s, dtype=torch.bfloat16) * 0.8)(B, Sk, Dc)),
            "w_q": _w((lambda *s: torch.randn(*s, dtype=torch.bfloat16) * 1.6)(M, H, D) * (M ** -0.5)),
            "w_uk": _w((lambda *s: torch.randn(*s, dtype=torch.bfloat16) * 1.6)(Dc, H, D) * (Dc ** -0.5)),
            "w_uv": _w((lambda *s: torch.randn(*s, dtype=torch.bfloat16) * 0.8)(Dc, H, D) * (Dc ** -0.5)),
            "w_o": _w((lambda *s: torch.randn(*s, dtype=torch.bfloat16) * 0.8)(H, D, M) * ((H * D) ** -0.5)),
        }
    _A_in = _A_setup()

    def _B_setup():
        B, S, D, F = 2, 1, 640, 1728
        return {
            "x": _w((lambda *s: torch.randn(*s, dtype=torch.bfloat16) * 0.05)(B, S, D)),
            "w1": _w((lambda *s: torch.randn(*s, dtype=torch.bfloat16) * 0.2)(D, F) * (2.0 / D) ** 0.5),
            "w3": _w((lambda *s: torch.randn(*s, dtype=torch.bfloat16) * 0.2)(D, F) * (2.0 / D) ** 0.5),
            "w2": _w((lambda *s: torch.randn(*s, dtype=torch.bfloat16) * 0.2)(F, D) * (2.0 / F) ** 0.5),
        }
    _B_in = _B_setup()

    return {
        "A_x": _A_in["x"],
        "A_c_kv": _A_in["c_kv"],
        "A_w_q": _A_in["w_q"],
        "A_w_uk": _A_in["w_uk"],
        "A_w_uv": _A_in["w_uv"],
        "A_w_o": _A_in["w_o"],
        "B_x": _B_in["x"],
        "B_w1": _B_in["w1"],
        "B_w3": _B_in["w3"],
        "B_w2": _B_in["w2"],
    }

def reference(A_x, A_c_kv, A_w_q, A_w_uk, A_w_uv, A_w_o, B_x, B_w1, B_w3, B_w2):
    def _A_ref(x, c_kv, w_q, w_uk, w_uv, w_o):
        B, Sq, M = x.shape[0], x.shape[1], x.shape[2]
        Sk = c_kv.shape[1]
        H, D = w_q.shape[1], w_q.shape[2]
        q = torch.einsum("bqm,mhd->bhqd", x, w_q)
        k = torch.einsum("bkc,chd->bhkd", c_kv, w_uk)
        v = torch.einsum("bkc,chd->bhkd", c_kv, w_uv)
        scores = torch.einsum("bhqd,bhkd->bhqk", q, k) * (D ** -0.5)
        off = Sk - Sq
        qi = torch.arange(Sq)[:, None]
        kj = torch.arange(Sk)[None, :]
        cmask = kj <= (qi + off)
        scores = torch.where(cmask[None, None], scores, -1e9)
        p = _softmax(scores, axis=-1)
        ctx = torch.einsum("bhqk,bhkd->bhqd", p, v)
        return torch.einsum("bhqd,hdm->bqm", ctx, w_o)

    def _B_ref(x, w1, w3, w2):
        gate = torch.einsum("bsd,df->bsf", x, w1)
        up = torch.einsum("bsd,df->bsf", x, w3)
        h = _silu(gate) * up
        out = torch.einsum("bsf,fd->bsd", h, w2)
        return out

    x = A_x
    h = x + _A_ref(_layernorm(x, axis=-1), A_c_kv, A_w_q, A_w_uk, A_w_uv, A_w_o)
    h2 = h + _B_ref(_layernorm(h, axis=-1), B_w1, B_w3, B_w2)
    return h2
```

```cpp
#include <hip/hip_runtime.h>
#include <hip/hip_bf16.h>
typedef unsigned short u16;
__device__ __forceinline__ float bf2f(u16 b){ return __uint_as_float(((unsigned)b)<<16); }
__device__ __forceinline__ u16 f2bf(float f){ __hip_bfloat16 h = __float2bfloat16(f); return *(u16*)&h; }

constexpr int B=2,H=32,SK=8192,D=128,M=640,DC=512,F=1728;
constexpr int NCH=128;

__device__ void layernorm2(const float* v  , float* out  , float* red){
  int t=threadIdx.x;
  for(int b=0;b<2;b++){
    float s=0; for(int i=t;i<M;i+=blockDim.x) s+=v[b*M+i];
    red[t]=s; __syncthreads();
    for(int o=blockDim.x/2;o>0;o>>=1){ if(t<o) red[t]+=red[t+o]; __syncthreads(); }
    float mean=red[0]/M; __syncthreads();
    float q=0; for(int i=t;i<M;i+=blockDim.x){ float d=v[b*M+i]-mean; q+=d*d; }
    red[t]=q; __syncthreads();
    for(int o=blockDim.x/2;o>0;o>>=1){ if(t<o) red[t]+=red[t+o]; __syncthreads(); }
    float rs=1.0f/sqrtf(red[0]/M+1e-5f); __syncthreads();
    for(int i=t;i<M;i+=blockDim.x) out[b*M+i]=(v[b*M+i]-mean)*rs;
    __syncthreads();
  }
}

__global__ void k_q(const u16* x,const u16* wq,float* q){
  __shared__ float xv[2*M], xn[2*M], red[256];
  int t=threadIdx.x;
  for(int i=t;i<2*M;i+=256) xv[i]=bf2f(x[i]);
  __syncthreads();
  layernorm2(xv,xn,red);
  int n=blockIdx.x*256+t;
  float a0=0,a1=0;
  for(int m=0;m<M;m++){ float w=bf2f(wq[(size_t)m*4096+n]); a0+=xn[m]*w; a1+=xn[M+m]*w; }
  q[n]=a0; q[4096+n]=a1;
}
__global__ void k_qabs(const float* q,const u16* wuk,float* qabs){
  int h=blockIdx.x, c=threadIdx.x;
  float a0=0,a1=0;
  for(int d=0;d<D;d++){ float w=bf2f(wuk[((size_t)c*H+h)*D+d]); a0+=q[h*D+d]*w; a1+=q[4096+h*D+d]*w; }
  const float sc=0.08838834764831845f;
  qabs[(0*H+h)*DC+c]=a0*sc; qabs[(1*H+h)*DC+c]=a1*sc;
}
__global__ void __launch_bounds__(256) k_attn(const u16* ckv,const float* qabs,float* opart,float* ml){
  __shared__ float s[H][64];
  __shared__ float mx[H], sm[H];
  int b=blockIdx.x/NCH, ch=blockIdx.x%NCH, t=threadIdx.x;
  const u16* kv=ckv+((size_t)b*SK+ch*64)*DC;
  for(int i=t;i<H*64;i+=256){ int h=i/64,k=i%64; float a=0; const float* qq=qabs+(b*H+h)*DC; const u16* kr=kv+(size_t)k*DC;
    for(int c=0;c<DC;c++) a+=qq[c]*bf2f(kr[c]); s[h][k]=a; }
  __syncthreads();
  if(t<H){ float m=-1e30f; for(int k=0;k<64;k++) m=fmaxf(m,s[t][k]); float l=0; for(int k=0;k<64;k++){ float p=__expf(s[t][k]-m); s[t][k]=p; l+=p;} mx[t]=m; sm[t]=l;
    ml[((b*NCH+ch)*H+t)*2]=m; ml[((b*NCH+ch)*H+t)*2+1]=l; }
  __syncthreads();
  for(int c=t;c<DC;c+=256){
#pragma unroll 1
 for(int h=0;h<H;h++){ float a=0;
#pragma unroll 4
 for(int k=0;k<64;k++) a+=s[h][k]*bf2f(kv[(size_t)k*DC+c]); opart[(((size_t)b*NCH+ch)*H+h)*DC+c]=a; } }
}
__global__ void k_comb(const float* opart,const float* ml,const u16* wuv,float* ctx){
  __shared__ float cc[DC];
  int b=blockIdx.x/H, h=blockIdx.x%H, t=threadIdx.x;
  float m=-1e30f; for(int ch=0;ch<NCH;ch++) m=fmaxf(m,ml[((b*NCH+ch)*H+h)*2]);
  float l=0,a=0; for(int ch=0;ch<NCH;ch++){ float w=__expf(ml[((b*NCH+ch)*H+h)*2]-m); l+=w*ml[((b*NCH+ch)*H+h)*2+1]; a+=w*opart[(((size_t)b*NCH+ch)*H+h)*DC+t]; }
  cc[t]=a/l; __syncthreads();
  if(t<D){ float s=0; for(int c=0;c<DC;c++) s+=cc[c]*bf2f(wuv[((size_t)c*H+h)*D+t]); ctx[b*4096+h*D+t]=s; }
}
__global__ void k_out(const float* ctx,const u16* wo,const u16* x,float* hbuf){
  __shared__ float r[2][4][64];
  int t=threadIdx.x, col=blockIdx.x*64+(t&63), ks=t>>6;
  float a0=0,a1=0;
  for(int k=ks*1024;k<ks*1024+1024;k++){ float w=bf2f(wo[(size_t)k*M+col]); a0+=ctx[k]*w; a1+=ctx[4096+k]*w; }
  r[0][ks][t&63]=a0; r[1][ks][t&63]=a1; __syncthreads();
  if(t<128){ int b=t>>6,c=t&63; int cl=blockIdx.x*64+c; hbuf[b*M+cl]=bf2f(x[b*M+cl])+r[b][0][c]+r[b][1][c]+r[b][2][c]+r[b][3][c]; }
}
__global__ void k_ffn1(const float* hbuf,const u16* w1,const u16* w3,float* hh){
  __shared__ float hv[2*M], hn[2*M], red[256];
  __shared__ float r[4][4][64];
  int t=threadIdx.x;
  for(int i=t;i<2*M;i+=256) hv[i]=hbuf[i];
  __syncthreads();
  layernorm2(hv,hn,red);
  int f=blockIdx.x*64+(t&63), ks=t>>6;
  float g0=0,g1=0,u0=0,u1=0;
  for(int k=ks*160;k<ks*160+160;k++){ float a=bf2f(w1[(size_t)k*F+f]), c=bf2f(w3[(size_t)k*F+f]); g0+=hn[k]*a; g1+=hn[M+k]*a; u0+=hn[k]*c; u1+=hn[M+k]*c; }
  r[0][ks][t&63]=g0; r[1][ks][t&63]=g1; r[2][ks][t&63]=u0; r[3][ks][t&63]=u1; __syncthreads();
  if(t<128){ int b=t>>6,c=t&63; float g=r[b][0][c]+r[b][1][c]+r[b][2][c]+r[b][3][c]; float u=r[2+b][0][c]+r[2+b][1][c]+r[2+b][2][c]+r[2+b][3][c];
    hh[b*F+blockIdx.x*64+c]=g/(1.0f+__expf(-g))*u; }
}
__global__ void k_ffn2(const float* hh,const u16* w2,const float* hbuf,u16* out){
  __shared__ float r[2][4][64];
  int t=threadIdx.x, col=blockIdx.x*64+(t&63), ks=t>>6;
  float a0=0,a1=0;
  for(int k=ks*432;k<ks*432+432;k++){ float w=bf2f(w2[(size_t)k*M+col]); a0+=hh[k]*w; a1+=hh[F+k]*w; }
  r[0][ks][t&63]=a0; r[1][ks][t&63]=a1; __syncthreads();
  if(t<128){ int b=t>>6,c=t&63; int cl=blockIdx.x*64+c; out[b*M+cl]=f2bf(hbuf[b*M+cl]+r[b][0][c]+r[b][1][c]+r[b][2][c]+r[b][3][c]); }
}

extern "C" void kernel_launch(void* const* d_in, const int* in_sizes, int n_in,
                              void* d_out, int out_size, void* d_ws, size_t ws_size,
                              hipStream_t stream) {
  const u16* x=(const u16*)d_in[0]; const u16* ckv=(const u16*)d_in[1];
  const u16* wq=(const u16*)d_in[2]; const u16* wuk=(const u16*)d_in[3]; const u16* wuv=(const u16*)d_in[4]; const u16* wo=(const u16*)d_in[5];
  const u16* w1=(const u16*)d_in[7]; const u16* w3=(const u16*)d_in[8]; const u16* w2=(const u16*)d_in[9];
  float* ws=(float*)d_ws;
  float* q=ws; float* qabs=q+8192; float* ml=qabs+2*H*DC; float* ctx=ml+2*NCH*H*2; float* hbuf=ctx+8192; float* hh=hbuf+2*M; float* opart=hh+2*F+128;
  k_q<<<16,256,0,stream>>>(x,wq,q);
  k_qabs<<<32,512,0,stream>>>(q,wuk,qabs);
  k_attn<<<256,256,0,stream>>>(ckv,qabs,opart,ml);
  k_comb<<<64,512,0,stream>>>(opart,ml,wuv,ctx);
  k_out<<<10,256,0,stream>>>(ctx,wo,x,hbuf);
  k_ffn1<<<27,256,0,stream>>>(hbuf,w1,w3,hh);
  k_ffn2<<<10,256,0,stream>>>(hh,w2,hbuf,(u16*)d_out);
}
```

```cpp
#include <hip/hip_runtime.h>
#include <stdint.h>

typedef unsigned short u16; typedef unsigned int u32; typedef unsigned long long u64;
typedef u32 v4u __attribute__((ext_vector_type(4)));
typedef u32 v2u __attribute__((ext_vector_type(2)));
typedef float f32x2 __attribute__((ext_vector_type(2)));
typedef float f32x4 __attribute__((ext_vector_type(4)));
typedef float f32x16 __attribute__((ext_vector_type(16)));
typedef short s16x4 __attribute__((ext_vector_type(4)));
typedef __bf16 b16x8 __attribute__((ext_vector_type(8)));
typedef __bf16 b16x2 __attribute__((ext_vector_type(2)));
#define GAS __attribute__((address_space(1)))
#define LAS __attribute__((address_space(3)))
typedef GAS u64 gu64;
#define RLX_AGENT __ATOMIC_RELAXED, __HIP_MEMORY_SCOPE_AGENT
#define DI __device__ __forceinline__

#define ATT_DELAY 80
#define FW_DELAY 450

constexpr int MM = 640, FF = 1728, SK = 8192;
constexpr int OFF_EPOCH = 0;
constexpr int OFF_READY = 1024;
constexpr int OFF_QG    = 4096;
constexpr int OFF_QA    = OFF_QG + 131072;
constexpr int OFF_ST    = OFF_QA + 131072;
constexpr int OFF_D     = OFF_ST + 262144;
constexpr int OFF_E     = OFF_D + 131072;
constexpr int OFF_FIN   = OFF_E + 20480;
constexpr int OFF_IN    = OFF_FIN + 40 * 108 * 128;
constexpr int WS_BYTES  = OFF_IN + 128 * 32768;
constexpr int OFF_STAMP = WS_BYTES;
constexpr u32 SENT = 0xFFFFFFFFu;
constexpr int SPIN_CAP = 1 << 16;

DI u64 rt() { return __builtin_amdgcn_s_memrealtime(); }
#define STAMP(i) do {} while (0)
#define STAMPW(i, w) do {} while (0)

DI v4u ld_nt(const void* p) { return __builtin_nontemporal_load((const GAS v4u*)p); }
DI v4u ld_sc1(__amdgpu_buffer_rsrc_t rs, int off) { return __builtin_amdgcn_raw_buffer_load_b128(rs, off, 0, 16); }
DI void st_sc1(__amdgpu_buffer_rsrc_t rs, int off, v4u v) { __builtin_amdgcn_raw_buffer_store_b128(v, rs, off, 0, 16); }
#define CBAR() asm volatile("" ::: "memory")
#define LBAR() asm volatile("s_waitcnt lgkmcnt(0)\n\ts_barrier" ::: "memory")
#define DRAIN() asm volatile("s_waitcnt vmcnt(0)" ::: "memory")
#define SB() __builtin_amdgcn_sched_barrier(0)

DI float bflo(u32 w) { return __uint_as_float(w << 16); }
DI float bfhi(u32 w) { return __uint_as_float(w & 0xFFFF0000u); }
DI u32 pk2(float a, float b) { f32x2 v = {a, b}; return __builtin_bit_cast(u32, __builtin_convertvector(v, b16x2)); }
DI bool has_sent(v4u v) { return (v.x == SENT) | (v.y == SENT) | (v.z == SENT) | (v.w == SENT); }
DI u32 nosent(u32 x) { return x < 0xFFFFFFFEu ? x : 0xFFFFFFFEu; }
DI v4u nosent4(f32x4 v) { return v4u{nosent(__float_as_uint(v.x)), nosent(__float_as_uint(v.y)), nosent(__float_as_uint(v.z)), nosent(__float_as_uint(v.w))}; }
DI f32x4 mfma32(v4u a, v4u b, f32x4 c) { return __builtin_amdgcn_mfma_f32_16x16x32_bf16(__builtin_bit_cast(b16x8, a), __builtin_bit_cast(b16x8, b), c, 0, 0, 0); }
DI f32x4 mfma16(v2u a, v2u b, f32x4 c) { return __builtin_amdgcn_mfma_f32_16x16x16bf16_1k(__builtin_bit_cast(s16x4, a), __builtin_bit_cast(s16x4, b), c, 0, 0, 0); }
DI f32x16 mfmaB(v4u a, v4u b, f32x16 c) { return __builtin_amdgcn_mfma_f32_32x32x16_bf16(__builtin_bit_cast(b16x8, a), __builtin_bit_cast(b16x8, b), c, 0, 0, 0); }
DI void tr8(const v4u (&L)[8], v4u (&T)[8]) {
#pragma unroll
  for (int dw = 0; dw < 4; ++dw)
#pragma unroll
    for (int p = 0; p < 4; ++p) {
      T[2 * dw][p]     = __builtin_amdgcn_perm(L[2 * p + 1][dw], L[2 * p][dw], 0x05040100u);
      T[2 * dw + 1][p] = __builtin_amdgcn_perm(L[2 * p + 1][dw], L[2 * p][dw], 0x07060302u);
    }
}
DI void tr4(const v4u (&L)[4], v2u (&T)[8]) {
#pragma unroll
  for (int dw = 0; dw < 4; ++dw)
#pragma unroll
    for (int p = 0; p < 2; ++p) {
      T[2 * dw][p]     = __builtin_amdgcn_perm(L[2 * p + 1][dw], L[2 * p][dw], 0x05040100u);
      T[2 * dw + 1][p] = __builtin_amdgcn_perm(L[2 * p + 1][dw], L[2 * p][dw], 0x07060302u);
    }
}
DI u32 split2(float a, float b, bool lo) {
  u32 h = pk2(a, b);
  u32 l = pk2(a - bflo(h), b - bfhi(h));
  return lo ? l : h;
}
template <int CTRL> DI float dpp(float v) { return __builtin_bit_cast(float, __builtin_amdgcn_update_dpp(0, __builtin_bit_cast(int, v), CTRL, 0xf, 0xf, true)); }
DI float wave_sum(float v) {
#pragma unroll
  for (int o = 32; o > 0; o >>= 1) v += __shfl_xor(v, o);
  return v;
}
DI int kvoff(int row, int ch) { return row * 1024 + 16 * (ch ^ (((row & 3) << 2) | ((row >> 2) & 3))); }

struct Params {
  const u16 *x, *ckv, *wq, *wuk, *wuv, *wo, *w1, *w3, *w2;
  u16* out;
  unsigned char* ws;
};

__global__ void __launch_bounds__(512, 2) mega(Params p) {
  extern __shared__ __attribute__((aligned(16))) unsigned char smem[];
  LAS unsigned char* L = (LAS unsigned char*)smem;
  const int bid = blockIdx.x, tid = threadIdx.x, lane = tid & 63;
  const int wave = __builtin_amdgcn_readfirstlane(tid >> 6);
  unsigned char* ws = p.ws;
  const __amdgpu_buffer_rsrc_t rs = __builtin_amdgcn_make_buffer_rsrc((void*)ws, 0, WS_BYTES, 0x00020000);
  const u64 t_start = rt();
  const u64 epoch = __hip_atomic_load((gu64*)(ws + OFF_EPOCH), RLX_AGENT);
  const int r16 = lane & 15, gq = lane >> 4;

  if (bid < 128) {
    constexpr int STW = 0;
    STAMP(0);
    const int g = bid, h = g >> 2, part = g & 3;
    LAS u16* XN = (LAS u16*)(L);
    LAS float* RED = (LAS float*)(L + 8192);
    LAS u16* QV = (LAS u16*)(L + 6144);
    v4u wk[4];
    const u16* wkp = p.wuk + ((size_t)(part * 128 + wave * 16 + r16) * 32 + h) * 128 + gq * 8;
    {
      const int dh = part & 1, mh = part >> 1;
      v4u Lq[8];
      if (wave < 5) {
        const int cb = lane & 7, ks = (lane >> 3) & 1;
        const u16* src = p.wq + (size_t)(mh * 320 + wave * 64 + ks * 32 + gq * 8) * 4096 + h * 128 + dh * 64 + cb * 8;
#pragma unroll
        for (int i = 0; i < 8; ++i) Lq[i] = ld_nt(src + (size_t)i * 4096);
        SB();
#pragma unroll
        for (int s = 0; s < 4; ++s) wk[s] = ld_nt(wkp + s * 32);
        SB();
      } else {
        if (wave < 7) {
          const int b = wave - 5;
          u32 xv[5];
#pragma unroll
          for (int i = 0; i < 5; ++i) xv[i] = ((const GAS u32*)(p.x + b * MM))[lane + 64 * i];
          float s = 0.f;
#pragma unroll
          for (int i = 0; i < 5; ++i) s += bflo(xv[i]) + bfhi(xv[i]);
          const float mean = wave_sum(s) * (1.0f / MM);
          float q = 0.f;
#pragma unroll
          for (int i = 0; i < 5; ++i) { float a = bflo(xv[i]) - mean, c = bfhi(xv[i]) - mean; q += a * a + c * c; }
          const float rstd = 1.0f / sqrtf(wave_sum(q) * (1.0f / MM) + 1e-5f);
#pragma unroll
          for (int i = 0; i < 5; ++i) {
            const float a = (bflo(xv[i]) - mean) * rstd, c = (bfhi(xv[i]) - mean) * rstd;
            ((LAS u32*)XN)[(0 * 2 + b) * 320 + lane + 64 * i] = split2(a, c, false);
            ((LAS u32*)XN)[(1 * 2 + b) * 320 + lane + 64 * i] = split2(a, c, true);
          }
        } else {
          __builtin_amdgcn_s_sleep(16);
        }
        SB();
#pragma unroll
        for (int s = 0; s < 4; ++s) wk[s] = ld_nt(wkp + s * 32);
        SB();
      }
      LBAR();
      if (wave < 5) {
        const int row = lane & 7;
        const v4u A = *(LAS v4u*)(XN + ((row & 1) * 2 + ((row >> 1) & 1)) * MM + mh * 320 + wave * 64 + (row >> 2) * 32 + gq * 8);
        v4u T[8];
        tr8(Lq, T);
        f32x4 acc[8];
#pragma unroll
        for (int i = 0; i < 8; ++i) acc[i] = mfma32(A, T[i], f32x4{0.f, 0.f, 0.f, 0.f});
        STAMP(1);
        if (lane < 8 || (lane >= 24 && lane < 32)) {
          const int ks = lane >> 4, cb = lane & 7;
#pragma unroll
          for (int b = 0; b < 2; ++b) {
            LAS f32x4* dst = (LAS f32x4*)(RED + ((wave * 2 + ks) * 2 + b) * 64 + cb * 8);
            dst[0] = f32x4{acc[0][2 * b] + acc[0][2 * b + 1], acc[1][2 * b] + acc[1][2 * b + 1], acc[2][2 * b] + acc[2][2 * b + 1], acc[3][2 * b] + acc[3][2 * b + 1]};
            dst[1] = f32x4{acc[4][2 * b] + acc[4][2 * b + 1], acc[5][2 * b] + acc[5][2 * b + 1], acc[6][2 * b] + acc[6][2 * b + 1], acc[7][2 * b] + acc[7][2 * b + 1]};
          }
        }
      }
      LBAR();
    }
    const u32 tag = (u32)epoch + 1u;
    if (wave >= 6) {
      const int t = tid - 384;
      float s = 0.f;
#pragma unroll
      for (int k = 0; k < 10; ++k) s += RED[k * 128 + t];
      __hip_atomic_store((gu64*)(ws + OFF_QG) + g * 128 + t, ((u64)tag << 32) | __float_as_uint(s), RLX_AGENT);
      STAMPW(2, 6);
    }
    if (wave < 4) {
      const int b = tid >> 7, d = tid & 127;
      gu64* q0 = (gu64*)(ws + OFF_QG) + (h * 4 + (d >> 6)) * 128 + b * 64 + (d & 63);
      u64 a, c;
      for (int sp = 0;; ++sp) {
        a = __hip_atomic_load(q0, RLX_AGENT);
        c = __hip_atomic_load(q0 + 256, RLX_AGENT);
        const bool ok = ((u32)(a >> 32) == tag) & ((u32)(c >> 32) == tag);
        if (__all(ok) || sp > SPIN_CAP) break;
      }
      const float q = (__uint_as_float((u32)a) + __uint_as_float((u32)c)) * (0.08838834764831845f * 1.4426950408889634f);
      const u32 hi = pk2(q, 0.f) & 0xFFFFu;
      const u32 lo = pk2(q - bflo(hi), 0.f) & 0xFFFFu;
      QV[(0 * 2 + b) * 128 + d] = (u16)hi;
      QV[(1 * 2 + b) * 128 + d] = (u16)lo;
      STAMP(3);
    }
    LBAR();
    {
      f32x4 acc = {0.f, 0.f, 0.f, 0.f};
      const LAS u16* qb = QV + ((lane & 1) * 2 + ((lane >> 1) & 1)) * 128 + gq * 8;
#pragma unroll
      for (int s = 0; s < 4; ++s) acc = mfma32(wk[s], *(const LAS v4u*)(qb + s * 32), acc);
      f32x4 o;
#pragma unroll
      for (int i = 0; i < 4; ++i) o[i] = acc[i] + dpp<0xB1>(acc[i]);
      if (r16 == 0 || r16 == 2) {
        const int b = r16 >> 1;
        const int c0 = part * 128 + wave * 16 + gq * 4;
        st_sc1(rs, OFF_QA + ((b * 32 + h) * 256 + (c0 >> 1)) * 8, v4u{pk2(o[0], o[1]), tag, pk2(o[2], o[3]), tag});
      }
      STAMP(4);
    }
    const int fL = ((g >> 3) >> 2) * 8 + (g & 7);
    const bool isF = fL < 27;
    const int fs = fL * 4 + ((g >> 3) & 3);
    {
      const v4u sv = {SENT, SENT, SENT, SENT};
#pragma unroll
      for (int i = 0; i < 4; ++i) st_sc1(rs, OFF_IN + g * 32768 + i * 8192 + tid * 16, sv);
      if (wave == 5) st_sc1(rs, OFF_D + g * 1024 + lane * 16, sv);
      if (wave == 6 && g < 20) st_sc1(rs, OFF_E + g * 1024 + lane * 16, sv);
      if (wave < 5 && isF) st_sc1(rs, OFF_FIN + ((wave * 8 + (lane >> 3)) * 108 + fs) * 128 + (lane & 7) * 16, sv);
      DRAIN();
      LBAR();
      if (tid == 0) __hip_atomic_store((gu64*)(ws + OFF_READY) + g, epoch + 1ull, RLX_AGENT);
      STAMP(5);
    }
    v4u Tv[8];
    v4u Tf[8];
    v2u T2[8];
    {
      v4u Lv[8], Lf[8], L2[4];
      if (wave < 4) {
        const u16* src = p.wuv + ((size_t)(part * 128 + wave * 32 + gq * 8) * 32 + h) * 128 + r16 * 8;
#pragma unroll
        for (int i = 0; i < 8; ++i) Lv[i] = ld_nt(src + (size_t)i * 4096);
      }
      SB();
      while (rt() < t_start + FW_DELAY) __builtin_amdgcn_s_sleep(2);
      if (isF) {
        if (wave < 5) {
          const int cb = r16 & 3, ks = r16 >> 2;
          const u16* src = ((cb & 2) ? p.w3 : p.w1) + (size_t)(wave * 128 + ks * 32 + gq * 8) * FF + fs * 16 + (cb & 1) * 8;
#pragma unroll
          for (int i = 0; i < 8; ++i) Lf[i] = ld_nt(src + (size_t)i * FF);
        }
        if (wave >= 3) {
          const u16* src = p.w2 + (size_t)(fs * 16 + gq * 4) * MM + (wave - 3) * 128 + r16 * 8;
#pragma unroll
          for (int i = 0; i < 4; ++i) L2[i] = ld_nt(src + (size_t)i * MM);
        }
      }
      SB();
      if (wave < 4) tr8(Lv, Tv);
      if (isF) {
        if (wave < 5) tr8(Lf, Tf);
        if (wave >= 3) tr4(L2, T2);
      }
    }
    LAS float* CS = (LAS float*)(L + 32768);
    if (isF && wave < 5) {
      const v4u ones = {0x3F803F80u, 0x3F803F80u, 0x3F803F80u, 0x3F803F80u};
      f32x4 a[8];
#pragma unroll
      for (int i = 0; i < 8; ++i) a[i] = mfma32(ones, Tf[i], f32x4{0.f, 0.f, 0.f, 0.f});
      if (lane < 16) {
        LAS f32x4* dst = (LAS f32x4*)(CS + (wave * 4 + (r16 >> 2)) * 32 + (r16 & 3) * 8);
        dst[0] = f32x4{a[0][0], a[1][0], a[2][0], a[3][0]};
        dst[1] = f32x4{a[4][0], a[5][0], a[6][0], a[7][0]};
      }
    }
    v4u xa = {0, 0, 0, 0};
    if (wave < 5) xa = *(const GAS v4u*)(p.x + ((r16 >> 1) & 1) * MM + wave * 128 + (r16 >> 2) * 32 + gq * 8);
    STAMP(6);
    LAS float* R1 = (LAS float*)(L + 8192);
    LAS float* ML = (LAS float*)(L + 6144);
    LAS float* R2 = (LAS float*)(L + 40960);
    {
      v4u sv;
      const int soff = OFF_ST + ((part * 128 + wave * 16 + r16) * 32 + h) * 16;
      for (int sp = 0;; ++sp) {
        CBAR();
        sv = ld_sc1(rs, soff);
        const bool ok = (sv.y == tag) & (sv.w == tag);
        if (__all(ok) || sp > SPIN_CAP) break;
        __builtin_amdgcn_s_sleep(1);
      }
      STAMP(7);
      v4u pv[4];
      const int ioff = OFF_IN + g * 32768 + (wave * 16 + gq * 4) * 256 + r16 * 16;
      CBAR();
#pragma unroll
      for (int i = 0; i < 4; ++i) pv[i] = ld_sc1(rs, ioff + i * 256);
      SB();
      const float m = __uint_as_float(sv.x), l = __uint_as_float(sv.z);
      float mw = m;
      mw = fmaxf(mw, __shfl_xor(mw, 1)); mw = fmaxf(mw, __shfl_xor(mw, 2)); mw = fmaxf(mw, __shfl_xor(mw, 4)); mw = fmaxf(mw, __shfl_xor(mw, 8));
      const float wsrc = __builtin_amdgcn_exp2f(m - mw);
      float lw = wsrc * l;
      lw += __shfl_xor(lw, 1); lw += __shfl_xor(lw, 2); lw += __shfl_xor(lw, 4); lw += __shfl_xor(lw, 8);
      float wi[4];
#pragma unroll
      for (int i = 0; i < 4; ++i) wi[i] = __shfl(wsrc, gq * 4 + i);
      for (int sp = 0;; ++sp) {
        const bool bad = has_sent(pv[0]) | has_sent(pv[1]) | has_sent(pv[2]) | has_sent(pv[3]);
        if (!__any(bad) || sp > SPIN_CAP) break;
        CBAR();
#pragma unroll
        for (int i = 0; i < 4; ++i) pv[i] = ld_sc1(rs, ioff + i * 256);
      }
      STAMP(8);
      float a[8];
#pragma unroll
      for (int e = 0; e < 8; ++e) a[e] = 0.f;
#pragma unroll
      for (int i = 0; i < 4; ++i)
#pragma unroll
        for (int dw = 0; dw < 4; ++dw) { a[2 * dw] += wi[i] * bflo(pv[i][dw]); a[2 * dw + 1] += wi[i] * bfhi(pv[i][dw]); }
      LAS f32x4* dst = (LAS f32x4*)(R1 + (wave * 4 + gq) * 128 + r16 * 8);
      dst[0] = f32x4{a[0], a[1], a[2], a[3]};
      dst[1] = f32x4{a[4], a[5], a[6], a[7]};
      if (lane == 0) { ML[wave * 2] = mw; ML[wave * 2 + 1] = lw; }
      LBAR();
      if (wave < 4) {
        const int b = r16 >> 3, pw = (r16 >> 1) & 3, hl = r16 & 1;
        const f32x4 ml0 = *(LAS f32x4*)(ML + b * 8), ml1 = *(LAS f32x4*)(ML + b * 8 + 4);
        const float M = fmaxf(fmaxf(ml0.x, ml0.z), fmaxf(ml1.x, ml1.z));
        const float e0 = __builtin_amdgcn_exp2f(ml0.x - M), e1 = __builtin_amdgcn_exp2f(ml0.z - M), e2 = __builtin_amdgcn_exp2f(ml1.x - M), e3 = __builtin_amdgcn_exp2f(ml1.z - M);
        const float den = e0 * ml0.y + e1 * ml0.w + e2 * ml1.y + e3 * ml1.w;
        const float sc = (pw == 0 ? e0 : pw == 1 ? e1 : pw == 2 ? e2 : e3) / den;
        f32x4 s0 = {0.f, 0.f, 0.f, 0.f}, s1 = {0.f, 0.f, 0.f, 0.f};
#pragma unroll
        for (int q = 0; q < 4; ++q) {
          const LAS f32x4* src = (const LAS f32x4*)(R1 + ((b * 4 + pw) * 4 + q) * 128 + wave * 32 + gq * 8);
          s0 += src[0]; s1 += src[1];
        }
        s0 *= sc; s1 *= sc;
        const v4u A = {split2(s0.x, s0.y, hl), split2(s0.z, s0.w, hl), split2(s1.x, s1.y, hl), split2(s1.z, s1.w, hl)};
        f32x4 acc[8];
#pragma unroll
        for (int i = 0; i < 8; ++i) acc[i] = mfma32(A, Tv[i], f32x4{0.f, 0.f, 0.f, 0.f});
        float o[8];
#pragma unroll
        for (int i = 0; i < 8; ++i) o[i] = (acc[i][0] + acc[i][1]) + (acc[i][2] + acc[i][3]);
        LAS f32x4* d2 = (LAS f32x4*)(R2 + (wave * 4 + gq) * 128 + r16 * 8);
        d2[0] = f32x4{o[0], o[1], o[2], o[3]};
        d2[1] = f32x4{o[4], o[5], o[6], o[7]};
      }
      LBAR();
      if (wave == 0) {
        const int b = lane >> 5, d4 = lane & 31;
        f32x4 s = {0.f, 0.f, 0.f, 0.f};
#pragma unroll
        for (int w = 0; w < 4; ++w)
#pragma unroll
          for (int q = 0; q < 2; ++q) s += *(const LAS f32x4*)(R2 + (w * 4 + b * 2 + q) * 128 + d4 * 4);
        st_sc1(rs, OFF_D + g * 1024 + lane * 16, nosent4(s));
      }
      STAMP(9);
    }
    if (isF) {
      LAS float* RF = (LAS float*)(L + 8192);
      LAS float* SS = (LAS float*)(L + 6144 + 256);
      LAS u16* HH = (LAS u16*)(L + 6144 + 512);
      if (wave < 5) {
        const int ks = r16 >> 2, b = (r16 >> 1) & 1, hl = r16 & 1;
        const int eoff = OFF_E + (b * MM + wave * 128 + ks * 32 + gq * 8) * 4;
        v4u ev[8];
        for (int sp = 0;; ++sp) {
          CBAR();
#pragma unroll
          for (int k = 0; k < 4; ++k) { ev[2 * k] = ld_sc1(rs, eoff + k * 5120); ev[2 * k + 1] = ld_sc1(rs, eoff + k * 5120 + 16); }
          bool bad = false;
#pragma unroll
          for (int k = 0; k < 8; ++k) bad |= has_sent(ev[k]);
          if (!__any(bad) || sp > SPIN_CAP) break;
          __builtin_amdgcn_s_sleep(1);
        }
        STAMP(10);
        f32x4 s0 = {bflo(xa.x), bfhi(xa.x), bflo(xa.y), bfhi(xa.y)}, s1 = {bflo(xa.z), bfhi(xa.z), bflo(xa.w), bfhi(xa.w)};
#pragma unroll
        for (int k = 0; k < 4; ++k) { s0 += __builtin_bit_cast(f32x4, ev[2 * k]); s1 += __builtin_bit_cast(f32x4, ev[2 * k + 1]); }
        const v4u A = {split2(s0.x, s0.y, hl), split2(s0.z, s0.w, hl), split2(s1.x, s1.y, hl), split2(s1.z, s1.w, hl)};
        f32x4 acc[8];
#pragma unroll
        for (int i = 0; i < 8; ++i) acc[i] = mfma32(A, Tf[i], f32x4{0.f, 0.f, 0.f, 0.f});
        float t1 = (s0.x + s0.y) + (s0.z + s0.w) + (s1.x + s1.y) + (s1.z + s1.w);
        float t2 = (s0.x * s0.x + s0.y * s0.y) + (s0.z * s0.z + s0.w * s0.w) + (s1.x * s1.x + s1.y * s1.y) + (s1.z * s1.z + s1.w * s1.w);
        t1 += __shfl_xor(t1, 4); t2 += __shfl_xor(t2, 4);
        t1 += __shfl_xor(t1, 8); t2 += __shfl_xor(t2, 8);
        t1 += __shfl_xor(t1, 16); t2 += __shfl_xor(t2, 16);
        t1 += __shfl_xor(t1, 32); t2 += __shfl_xor(t2, 32);
        if (lane == 0 || lane == 2) { SS[(wave * 2 + (lane >> 1)) * 2] = t1; SS[(wave * 2 + (lane >> 1)) * 2 + 1] = t2; }
        if (gq == (r16 >> 2)) {
          const int cb = r16 & 3;
#pragma unroll
          for (int bb = 0; bb < 2; ++bb) {
            LAS f32x4* dst = (LAS f32x4*)(RF + ((wave * 4 + gq) * 2 + bb) * 32 + cb * 8);
            dst[0] = f32x4{acc[0][2 * bb] + acc[0][2 * bb + 1], acc[1][2 * bb] + acc[1][2 * bb + 1], acc[2][2 * bb] + acc[2][2 * bb + 1], acc[3][2 * bb] + acc[3][2 * bb + 1]};
            dst[1] = f32x4{acc[4][2 * bb] + acc[4][2 * bb + 1], acc[5][2 * bb] + acc[5][2 * bb + 1], acc[6][2 * bb] + acc[6][2 * bb + 1], acc[7][2 * bb] + acc[7][2 * bb + 1]};
          }
        }
      }
      float cs = 0.f;
      if (wave == 7) {
#pragma unroll
        for (int k = 0; k < 20; ++k) cs += CS[k * 32 + (lane & 31)];
      }
      LBAR();
      if (wave == 7) {
        const int b = lane >> 5, c = lane & 31;
        float s = 0.f;
#pragma unroll
        for (int k = 0; k < 20; ++k) s += RF[(k * 2 + b) * 32 + c];
        float t1 = 0.f, t2 = 0.f;
#pragma unroll
        for (int w = 0; w < 5; ++w) { t1 += SS[(w * 2 + b) * 2]; t2 += SS[(w * 2 + b) * 2 + 1]; }
        const float mean = t1 * (1.0f / MM);
        const float var = fmaxf(t2 * (1.0f / MM) - mean * mean, 0.f);
        const float rstd = 1.0f / sqrtf(var + 1e-5f);
        const float v = (s - mean * cs) * rstd;
        const float u = __shfl_xor(v, 16);
        const float hh = v / (1.0f + __expf(-v)) * u;
        if (c < 16) {
          const u32 hi = pk2(hh, 0.f) & 0xFFFFu;
          const u32 lo = pk2(hh - bflo(hi), 0.f) & 0xFFFFu;
          HH[(b * 2 + 0) * 16 + c] = (u16)hi;
          HH[(b * 2 + 1) * 16 + c] = (u16)lo;
        }
      }
      LBAR();
      if (wave >= 3) {
        const v2u A = *(const LAS v2u*)(HH + (r16 & 3) * 16 + gq * 4);
        f32x4 acc[8];
#pragma unroll
        for (int i = 0; i < 8; ++i) acc[i] = mfma16(A, T2[i], f32x4{0.f, 0.f, 0.f, 0.f});
        const int b = gq >> 1, hf = gq & 1;
        f32x4 o;
#pragma unroll
        for (int i = 0; i < 4; ++i) {
          const float l0 = acc[i][0] + acc[i][1], l1 = acc[i][2] + acc[i][3];
          const float h0 = acc[4 + i][0] + acc[4 + i][1], h1 = acc[4 + i][2] + acc[4 + i][3];
          const float vl = b ? l1 : l0, vh = b ? h1 : h0;
          o[i] = hf ? vh : vl;
        }
        const int col = (wave - 3) * 128 + r16 * 8 + hf * 4;
        const int red = b * 20 + (col >> 5);
        st_sc1(rs, OFF_FIN + (red * 108 + fs) * 128 + (col & 31) * 4, nosent4(o));
        STAMPW(11, 3);
      }
    }
  } else {
    constexpr int STW = 4;
    STAMP(0);
    const int a = bid - 128;
    const int b = a >> 6;
    while (rt() < t_start + ATT_DELAY) __builtin_amdgcn_s_sleep(1);
    {
      const u16* base = p.ckv + (size_t)a * 128 * 512;
#pragma unroll
      for (int i = 0; i < 16; ++i) {
        const int row = wave * 16 + i;
        const int f = ((row & 3) << 2) | ((row >> 2) & 3);
        const u16* src = base + row * 512 + ((lane ^ f) * 8);
        unsigned keep;
        asm volatile("s_mov_b32 %0, m0\n\ts_mov_b32 m0, %2\n\ts_nop 0\n\tglobal_load_lds_dwordx4 %1, off nt\n\ts_mov_b32 m0, %0"
                     : "=&s"(keep) : "v"(src), "s"(row * 1024) : "memory");
      }
    }
    STAMP(1);
    v4u qv[8];
    const int qoff = OFF_QA + b * 65536 + tid * 16;
#pragma unroll
    for (int i = 0; i < 8; ++i) qv[i] = ld_sc1(rs, qoff + i * 8192);
    SB();
    asm volatile("s_waitcnt vmcnt(8)\n\ts_barrier" ::: "memory");
    STAMP(2);
    const u32 tag = (u32)epoch + 1u;
    const int r32 = lane & 31, hf = lane >> 5;
    v4u fr[32];
    if (wave < 4) {
      const int row = wave * 32 + r32;
#pragma unroll
      for (int s = 0; s < 32; ++s) fr[s] = *(const LAS v4u*)(L + kvoff(row, 2 * s + hf));
    } else {
      const int u = wave - 4;
      const int blk = (lane >> 4) & 1, q = (lane >> 2) & 3, pp = lane & 3;
      const int rg = blk * 4 + pp;
      const int cbase = ((rg & 1) * 16 + (rg >> 1) * 4);
#pragma unroll
      for (int t = 0; t < 4; ++t)
#pragma unroll
        for (int ks = 0; ks < 8; ++ks) {
          const int c = u * 128 + t * 32 + cbase;
          const int row0 = ks * 16 + 4 * hf + q;
          const s16x4 lo = __builtin_amdgcn_ds_read_tr16_b64_v4i16((LAS s16x4*)(L + kvoff(row0, c >> 3) + (c & 4) * 2));
          const s16x4 hi = __builtin_amdgcn_ds_read_tr16_b64_v4i16((LAS s16x4*)(L + kvoff(row0 + 8, c >> 3) + (c & 4) * 2));
          const v2u l2 = __builtin_bit_cast(v2u, lo), h2 = __builtin_bit_cast(v2u, hi);
          fr[t * 8 + ks] = v4u{l2.x, l2.y, h2.x, h2.y};
        }
    }
    SB();
    STAMP(3);
    for (int sp = 0;; ++sp) {
      bool ok = true;
#pragma unroll
      for (int i = 0; i < 8; ++i) ok &= (qv[i].y == tag) & (qv[i].w == tag);
      if (__all(ok) || sp > SPIN_CAP) break;
      CBAR();
#pragma unroll
      for (int i = 0; i < 8; ++i) qv[i] = ld_sc1(rs, qoff + i * 8192);
    }
    STAMP(4);
    LAS unsigned char* QA = L + 131072;
#pragma unroll
    for (int i = 0; i < 8; ++i) {
      const int gp = i * 512 + tid, hd = gp >> 7, c = (gp & 127) * 4;
      *(LAS v2u*)(QA + kvoff(hd, c >> 3) + (c & 4) * 2) = v2u{qv[i].x, qv[i].z};
    }
    LBAR();
    STAMP(5);
    LAS float* TM = (LAS float*)(L);
    LAS float* LS = (LAS float*)(L + 512);
    LAS v4u* PB = (LAS v4u*)(L + 2048);
    if (wave < 4) {
      f32x16 s;
#pragma unroll
      for (int i = 0; i < 16; ++i) s[i] = 0.f;
#pragma unroll
      for (int k = 0; k < 32; ++k) s = mfmaB(fr[k], *(const LAS v4u*)(QA + kvoff(r32, 2 * k + hf)), s);
      float m = s[0];
#pragma unroll
      for (int i = 1; i < 16; ++i) m = fmaxf(m, s[i]);
      m = fmaxf(m, __shfl_xor(m, 32));
      if (lane < 32) TM[wave * 32 + lane] = m;
      STAMPW(6, 0);
      LBAR();
      m = fmaxf(fmaxf(TM[r32], TM[32 + r32]), fmaxf(TM[64 + r32], TM[96 + r32]));
      float l = 0.f;
#pragma unroll
      for (int i = 0; i < 16; ++i) { s[i] = __builtin_amdgcn_exp2f(s[i] - m); l += s[i]; }
      PB[(wave * 2 + 0) * 64 + lane] = v4u{pk2(s[0], s[1]), pk2(s[2], s[3]), pk2(s[4], s[5]), pk2(s[6], s[7])};
      PB[(wave * 2 + 1) * 64 + lane] = v4u{pk2(s[8], s[9]), pk2(s[10], s[11]), pk2(s[12], s[13]), pk2(s[14], s[15])};
      LS[wave * 64 + lane] = l;
      LBAR();
    } else {
      const int u = wave - 4;
      for (int sp = 0;; ++sp) {
        CBAR();
        const v4u rv = ld_sc1(rs, OFF_READY + lane * 16);
        const u32 tl = (u32)(epoch + 1ull), th = (u32)((epoch + 1ull) >> 32);
        const bool ok = (rv.x == tl) & (rv.y == th) & (rv.z == tl) & (rv.w == th);
        if (__all(ok) || sp > SPIN_CAP) break;
        __builtin_amdgcn_s_sleep(1);
      }
      LBAR();
      LBAR();
      STAMP(7);
      v4u pf[8];
#pragma unroll
      for (int k = 0; k < 8; ++k) pf[k] = PB[k * 64 + lane];
      LAS unsigned char* OT = L + 16384 + u * 8704;
#pragma unroll
      for (int t = 0; t < 4; ++t) {
        f32x16 o;
#pragma unroll
        for (int i = 0; i < 16; ++i) o[i] = 0.f;
#pragma unroll
        for (int k = 0; k < 8; ++k) o = mfmaB(fr[t * 8 + k], pf[k], o);
        LAS v4u* dst = (LAS v4u*)(OT + r32 * 272 + t * 64 + hf * 32);
        dst[0] = v4u{nosent(pk2(o[0], o[1])), nosent(pk2(o[2], o[3])), nosent(pk2(o[4], o[5])), nosent(pk2(o[6], o[7]))};
        dst[1] = v4u{nosent(pk2(o[8], o[9])), nosent(pk2(o[10], o[11])), nosent(pk2(o[12], o[13])), nosent(pk2(o[14], o[15]))};
      }
      STAMP(8);
#pragma unroll
      for (int i = 0; i < 8; ++i) {
        const int hd = i * 4 + gq;
        const v4u v = *(const LAS v4u*)(OT + hd * 272 + r16 * 16);
        st_sc1(rs, OFF_IN + (hd * 4 + u) * 32768 + a * 256 + r16 * 16, v);
      }
      if (lane < 32) {
        const float m = fmaxf(fmaxf(TM[lane], TM[32 + lane]), fmaxf(TM[64 + lane], TM[96 + lane]));
        float l = 0.f;
#pragma unroll
        for (int k = 0; k < 8; ++k) l += LS[k * 32 + lane];
        st_sc1(rs, OFF_ST + ((u * 128 + a) * 32 + lane) * 16, v4u{__float_as_uint(m), tag, __float_as_uint(l), tag});
      }
      STAMP(9);
    }
    const int ai = a >> 3;
    if (ai < 10) {
      const int pr = (ai >> 1) * 8 + (a & 7);
      const int ksl = pr / 10, ms = (pr % 10) * 2 + (ai & 1);
      const int hd = ksl * 8 + wave;
      const int cb = r16 & 3, ks = r16 >> 2;
      v4u T[8];
      {
        v4u Lo[8];
        const u16* src = p.wo + (size_t)(hd * 128 + ks * 32 + gq * 8) * MM + ms * 32 + cb * 8;
#pragma unroll
        for (int i = 0; i < 8; ++i) Lo[i] = ld_nt(src + (size_t)i * MM);
        SB();
        tr8(Lo, T);
      }
      STAMP(10);
      const int bb = (r16 >> 1) & 1, hl = r16 & 1;
      const int doff = OFF_D + hd * 4096 + (bb * 128 + ks * 32 + gq * 8) * 4;
      v4u dv[8];
      for (int sp = 0;; ++sp) {
        CBAR();
#pragma unroll
        for (int k = 0; k < 4; ++k) { dv[2 * k] = ld_sc1(rs, doff + k * 1024); dv[2 * k + 1] = ld_sc1(rs, doff + k * 1024 + 16); }
        bool bad = false;
#pragma unroll
        for (int k = 0; k < 8; ++k) bad |= has_sent(dv[k]);
        if (!__any(bad) || sp > SPIN_CAP) break;
        __builtin_amdgcn_s_sleep(1);
      }
      STAMP(11);
      f32x4 s0 = {0.f, 0.f, 0.f, 0.f}, s1 = {0.f, 0.f, 0.f, 0.f};
#pragma unroll
      for (int k = 0; k < 4; ++k) { s0 += __builtin_bit_cast(f32x4, dv[2 * k]); s1 += __builtin_bit_cast(f32x4, dv[2 * k + 1]); }
      const v4u A = {split2(s0.x, s0.y, hl), split2(s0.z, s0.w, hl), split2(s1.x, s1.y, hl), split2(s1.z, s1.w, hl)};
      f32x4 acc[8];
#pragma unroll
      for (int i = 0; i < 8; ++i) acc[i] = mfma32(A, T[i], f32x4{0.f, 0.f, 0.f, 0.f});
      LAS float* RO = (LAS float*)(L + 65536);
      if (gq == ks) {
#pragma unroll
        for (int q = 0; q < 2; ++q) {
          LAS f32x4* dst = (LAS f32x4*)(RO + ((wave * 4 + gq) * 2 + q) * 32 + cb * 8);
          dst[0] = f32x4{acc[0][2 * q] + acc[0][2 * q + 1], acc[1][2 * q] + acc[1][2 * q + 1], acc[2][2 * q] + acc[2][2 * q + 1], acc[3][2 * q] + acc[3][2 * q + 1]};
          dst[1] = f32x4{acc[4][2 * q] + acc[4][2 * q + 1], acc[5][2 * q] + acc[5][2 * q + 1], acc[6][2 * q] + acc[6][2 * q + 1], acc[7][2 * q] + acc[7][2 * q + 1]};
        }
      }
      LBAR();
      if (wave == 4) {
        f32x4 s = {0.f, 0.f, 0.f, 0.f};
#pragma unroll
        for (int k = 0; k < 8; ++k) s += *(const LAS f32x4*)(RO + (gq * 8 + k) * 64 + r16 * 4);
#pragma unroll
        for (int i = 0; i < 4; ++i) { s[i] += __shfl_xor(s[i], 16); s[i] += __shfl_xor(s[i], 32); }
        if (lane < 16) st_sc1(rs, OFF_E + ((ksl * 2 + (r16 >> 3)) * MM + ms * 32 + (r16 & 7) * 4) * 4, nosent4(s));
      }
      STAMP(12);
    } else if (ai < 15) {
      const int red = (ai - 10) * 8 + (a & 7);
      const int bb = red / 20, c0 = (red % 20) * 32;
      LAS float* RFN = (LAS float*)(L + 65536);
      const int c4 = tid & 7, srcs = tid >> 3;
      const int foff = OFF_FIN + (red * 108 + srcs) * 128 + c4 * 16;
      const bool two = srcs < 44;
      const bool isE = srcs >= 44 && srcs < 48;
      const int off2 = two ? foff + 64 * 128 : (isE ? OFF_E + (((srcs - 44) * 2 + bb) * MM + c0 + c4 * 4) * 4 : foff);
      v4u f0, f1;
      for (int sp = 0;; ++sp) {
        CBAR();
        f0 = ld_sc1(rs, foff);
        f1 = ld_sc1(rs, off2);
        const bool bad = has_sent(f0) | has_sent(f1);
        if (!__any(bad) || sp > SPIN_CAP) break;
        __builtin_amdgcn_s_sleep(2);
      }
      STAMP(13);
      f32x4 s = __builtin_bit_cast(f32x4, f0);
      if (two || isE) s += __builtin_bit_cast(f32x4, f1);
#pragma unroll
      for (int i = 0; i < 4; ++i) { s[i] += __shfl_xor(s[i], 8); s[i] += __shfl_xor(s[i], 16); s[i] += __shfl_xor(s[i], 32); }
      if (lane < 8) *(LAS f32x4*)(RFN + wave * 32 + lane * 4) = s;
      LBAR();
      if (wave == 4 && lane < 16) {
        const u32 xw = ((const GAS u32*)(p.x + bb * MM + c0))[lane];
        float o0 = bflo(xw), o1 = bfhi(xw);
#pragma unroll
        for (int w = 0; w < 8; ++w) { o0 += RFN[w * 32 + 2 * lane]; o1 += RFN[w * 32 + 2 * lane + 1]; }
        ((GAS u32*)(p.out + bb * MM + c0))[lane] = pk2(o0, o1);
        if (red == 0 && lane == 0) __hip_atomic_store((gu64*)(ws + OFF_EPOCH), epoch + 1ull, RLX_AGENT);
      }
      STAMP(14);
    }
  }
}

extern "C" void kernel_launch(void* const* d_in, const int* in_sizes, int n_in, void* d_out, int out_size, void* d_ws, size_t ws_size,
                              hipStream_t stream) {
  Params p;
  p.x = (const u16*)d_in[0]; p.ckv = (const u16*)d_in[1]; p.wq = (const u16*)d_in[2]; p.wuk = (const u16*)d_in[3];
  p.wuv = (const u16*)d_in[4]; p.wo = (const u16*)d_in[5]; p.w1 = (const u16*)d_in[7]; p.w3 = (const u16*)d_in[8]; p.w2 = (const u16*)d_in[9];
  p.out = (u16*)d_out; p.ws = (unsigned char*)d_ws;
  (void)hipFuncSetAttribute((const void*)mega, hipFuncAttributeMaxDynamicSharedMemorySize, 163840);
  mega<<<256, 512, 163840, stream>>>(p);
}
```

```cpp
#include <hip/hip_runtime.h>
#include <stdint.h>
#include <stdio.h>

typedef unsigned short u16; typedef unsigned int u32; typedef unsigned long long u64;
typedef u32 v4u __attribute__((ext_vector_type(4)));
typedef u32 v2u __attribute__((ext_vector_type(2)));
typedef float f32x2 __attribute__((ext_vector_type(2)));
typedef float f32x4 __attribute__((ext_vector_type(4)));
typedef float f32x16 __attribute__((ext_vector_type(16)));
typedef short s16x4 __attribute__((ext_vector_type(4)));
typedef __bf16 b16x8 __attribute__((ext_vector_type(8)));
typedef __bf16 b16x2 __attribute__((ext_vector_type(2)));
typedef __amdgpu_buffer_rsrc_t rsrc_t;
#define GAS __attribute__((address_space(1)))
#define LAS __attribute__((address_space(3)))
typedef GAS u64 gu64;
#define RLX_AGENT __ATOMIC_RELAXED, __HIP_MEMORY_SCOPE_AGENT
#define DI __device__ __forceinline__

#define ATT_DELAY 80
#define FW_DELAY 0
#define FW_AUX 2
#define WO_AUX 2

constexpr int MM = 640, FF = 1728;
constexpr int OFF_EPOCH = 0;
constexpr int OFF_READY = 1024;
constexpr int OFF_QG    = 4096;
constexpr int OFF_QA    = OFF_QG + 131072;
constexpr int OFF_ST    = OFF_QA + 131072;
constexpr int OFF_D     = OFF_ST + 262144;
constexpr int OFF_E     = OFF_D + 131072;
constexpr int OFF_FIN   = OFF_E + 20480;
constexpr int OFF_IN    = OFF_FIN + 40 * 108 * 128;
constexpr int WS_BYTES  = OFF_IN + 128 * 32768;
constexpr int OFF_STAMP = WS_BYTES;
constexpr u32 SENT = 0xFFFFFFFFu;
constexpr int SPIN_CAP = 1 << 16;
constexpr int OOB = 0x40000000;

DI u64 rt() { return __builtin_amdgcn_s_memrealtime(); }
#define STAMP(i) do {} while (0)
#define STAMP_FLUSH() do {} while (0)

DI rsrc_t mk(const void* p, int bytes) { return __builtin_amdgcn_make_buffer_rsrc((void*)p, 0, bytes, 0x00020000); }
template <int AUX> DI v4u ldw(rsrc_t rs, int off) { return __builtin_amdgcn_raw_buffer_load_b128(rs, off, 0, AUX); }
DI v4u ld_sc1(rsrc_t rs, int off) { return __builtin_amdgcn_raw_buffer_load_b128(rs, off, 0, 16); }
DI void st_sc1(rsrc_t rs, int off, v4u v) { __builtin_amdgcn_raw_buffer_store_b128(v, rs, off, 0, 16); }
#define CBAR() asm volatile("" ::: "memory")
#define LBAR() asm volatile("s_waitcnt lgkmcnt(0)\n\ts_barrier" ::: "memory")
#define DRAIN() asm volatile("s_waitcnt vmcnt(0)" ::: "memory")
#define SB() __builtin_amdgcn_sched_barrier(0)

DI float bflo(u32 w) { return __uint_as_float(w << 16); }
DI float bfhi(u32 w) { return __uint_as_float(w & 0xFFFF0000u); }
DI u32 pk2(float a, float b) { f32x2 v = {a, b}; return __builtin_bit_cast(u32, __builtin_convertvector(v, b16x2)); }
DI u32 umax4(v4u v) { u32 a = v.x > v.y ? v.x : v.y, b = v.z > v.w ? v.z : v.w; return a > b ? a : b; }
DI u32 umax2(u32 a, u32 b) { return a > b ? a : b; }
DI u32 nosent(u32 x) { return x < 0xFFFFFFFEu ? x : 0xFFFFFFFEu; }
DI v4u nosent4(f32x4 v) { return v4u{nosent(__float_as_uint(v.x)), nosent(__float_as_uint(v.y)), nosent(__float_as_uint(v.z)), nosent(__float_as_uint(v.w))}; }
DI f32x4 mfma32(v4u a, v4u b, f32x4 c) { return __builtin_amdgcn_mfma_f32_16x16x32_bf16(__builtin_bit_cast(b16x8, a), __builtin_bit_cast(b16x8, b), c, 0, 0, 0); }
DI f32x4 mfma16(v2u a, v2u b, f32x4 c) { return __builtin_amdgcn_mfma_f32_16x16x16bf16_1k(__builtin_bit_cast(s16x4, a), __builtin_bit_cast(s16x4, b), c, 0, 0, 0); }
DI f32x16 mfmaB(v4u a, v4u b, f32x16 c) { return __builtin_amdgcn_mfma_f32_32x32x16_bf16(__builtin_bit_cast(b16x8, a), __builtin_bit_cast(b16x8, b), c, 0, 0, 0); }
DI void tr8(const v4u (&L)[8], v4u (&T)[8]) {
#pragma unroll
  for (int dw = 0; dw < 4; ++dw)
#pragma unroll
    for (int p = 0; p < 4; ++p) {
      T[2 * dw][p]     = __builtin_amdgcn_perm(L[2 * p + 1][dw], L[2 * p][dw], 0x05040100u);
      T[2 * dw + 1][p] = __builtin_amdgcn_perm(L[2 * p + 1][dw], L[2 * p][dw], 0x07060302u);
    }
}
DI void tr4(const v4u (&L)[4], v2u (&T)[8]) {
#pragma unroll
  for (int dw = 0; dw < 4; ++dw)
#pragma unroll
    for (int p = 0; p < 2; ++p) {
      T[2 * dw][p]     = __builtin_amdgcn_perm(L[2 * p + 1][dw], L[2 * p][dw], 0x05040100u);
      T[2 * dw + 1][p] = __builtin_amdgcn_perm(L[2 * p + 1][dw], L[2 * p][dw], 0x07060302u);
    }
}
DI u32 split2(float a, float b, bool lo) {
  u32 h = pk2(a, b);
  u32 l = pk2(a - bflo(h), b - bfhi(h));
  return lo ? l : h;
}
template <int CTRL> DI float dpp(float v) { return __builtin_bit_cast(float, __builtin_amdgcn_update_dpp(0, __builtin_bit_cast(int, v), CTRL, 0xf, 0xf, true)); }
DI float row_sum16(float v) { v += dpp<0xB1>(v); v += dpp<0x4E>(v); v += dpp<0x141>(v); v += dpp<0x140>(v); return v; }
DI float red32(float a, float b) { auto r = __builtin_amdgcn_permlane32_swap(__float_as_uint(a), __float_as_uint(b), false, false); return __uint_as_float(r[0]) + __uint_as_float(r[1]); }
DI float red16(float a, float b) { auto r = __builtin_amdgcn_permlane16_swap(__float_as_uint(a), __float_as_uint(b), false, false); return __uint_as_float(r[0]) + __uint_as_float(r[1]); }
DI float max32(float a) { auto r = __builtin_amdgcn_permlane32_swap(__float_as_uint(a), __float_as_uint(a), false, false); return fmaxf(__uint_as_float(r[0]), __uint_as_float(r[1])); }
DI float max16(float a) { auto r = __builtin_amdgcn_permlane16_swap(__float_as_uint(a), __float_as_uint(a), false, false); return fmaxf(__uint_as_float(r[0]), __uint_as_float(r[1])); }
DI float rdl(float v, int l) { return __builtin_bit_cast(float, __builtin_amdgcn_readlane(__builtin_bit_cast(int, v), l)); }
DI int kvoff(int row, int ch) { return row * 1024 + 16 * (ch ^ (((row & 3) << 2) | ((row >> 2) & 3))); }
DI void st_pairs(LAS float* dst, const f32x4 (&acc)[8], int q) {
  ((LAS f32x4*)dst)[0] = f32x4{acc[0][2 * q] + acc[0][2 * q + 1], acc[1][2 * q] + acc[1][2 * q + 1], acc[2][2 * q] + acc[2][2 * q + 1], acc[3][2 * q] + acc[3][2 * q + 1]};
  ((LAS f32x4*)dst)[1] = f32x4{acc[4][2 * q] + acc[4][2 * q + 1], acc[5][2 * q] + acc[5][2 * q + 1], acc[6][2 * q] + acc[6][2 * q + 1], acc[7][2 * q] + acc[7][2 * q + 1]};
}

struct Params {
  const u16 *x, *ckv, *wq, *wuk, *wuv, *wo, *w1, *w3, *w2;
  u16* out;
  unsigned char* ws;
};

__global__ void __launch_bounds__(512, 2) mega(Params p) {
  extern __shared__ __attribute__((aligned(16))) unsigned char smem[];
  LAS unsigned char* L = (LAS unsigned char*)smem;
  const int bid = blockIdx.x, tid = threadIdx.x, lane = tid & 63;
  const int wave = __builtin_amdgcn_readfirstlane(tid >> 6);
  unsigned char* ws = p.ws;
  const rsrc_t rs = mk(ws, WS_BYTES);
  const u64 t_start = rt();
  int stampv = 0;
  STAMP(0);
  const int r16 = lane & 15, gq = lane >> 4;
  const f32x4 Z4 = {0.f, 0.f, 0.f, 0.f};

  if (bid < 128) {
    const int g = bid, h = g >> 2, part = g & 3;
    LAS float* RED = (LAS float*)(L + 8192);
    LAS u16* QV = (LAS u16*)(L + 6144);
    const rsrc_t rx = mk(p.x, 2560);
    v4u wk[4];
    u32 tag; u64 epoch;
    {
      const int dh = part & 1, mh = part >> 1;
      const int oob = wave < 5 ? 0 : OOB;
      v2u xs[5];
#pragma unroll
      for (int i = 0; i < 5; ++i) xs[i] = __builtin_amdgcn_raw_buffer_load_b64(rx, (lane >> 5) * 1280 + ((lane & 31) + 32 * i) * 8, 0, 0);
      const int row = lane & 7, rb = (row >> 1) & 1;
      const v4u xa = ldw<0>(rx, oob + (rb * MM + mh * 320 + wave * 64 + (row >> 2) * 32 + gq * 8) * 2);
      v4u Lq[8];
      {
        const rsrc_t rq = mk(p.wq, 640 * 4096 * 2);
        const int off = oob + ((mh * 320 + wave * 64 + ((lane >> 3) & 1) * 32 + gq * 8) * 4096 + h * 128 + dh * 64 + (lane & 7) * 8) * 2;
#pragma unroll
        for (int i = 0; i < 8; ++i) Lq[i] = ldw<2>(rq, off + i * 8192);
      }
      epoch = __hip_atomic_load((gu64*)(ws + OFF_EPOCH), RLX_AGENT);
      {
        const rsrc_t rk = mk(p.wuk, 512 * 4096 * 2);
        const int off = (((part * 128 + wave * 16 + r16) * 32 + h) * 128 + gq * 8) * 2;
#pragma unroll
        for (int s = 0; s < 4; ++s) wk[s] = ldw<2>(rk, off + s * 64);
      }
      SB();
      float s = 0.f;
#pragma unroll
      for (int i = 0; i < 5; ++i) s += (bflo(xs[i].x) + bfhi(xs[i].x)) + (bflo(xs[i].y) + bfhi(xs[i].y));
      s = row_sum16(s); s = red16(s, s);
      const float mean = s * (1.0f / MM);
      float q = 0.f;
#pragma unroll
      for (int i = 0; i < 5; ++i) {
        const float a = bflo(xs[i].x) - mean, b = bfhi(xs[i].x) - mean, c = bflo(xs[i].y) - mean, d = bfhi(xs[i].y) - mean;
        q += (a * a + b * b) + (c * c + d * d);
      }
      q = row_sum16(q); q = red16(q, q);
      const float rstd = 1.0f / sqrtf(q * (1.0f / MM) + 1e-5f);
      const float m0 = rdl(mean, 0), m1 = rdl(mean, 32), r0 = rdl(rstd, 0), r1 = rdl(rstd, 32);
      const float mu = rb ? m1 : m0, rr = rb ? r1 : r0;
      const bool hl = row & 1;
      v4u A;
#pragma unroll
      for (int i = 0; i < 4; ++i) A[i] = split2((bflo(xa[i]) - mu) * rr, (bfhi(xa[i]) - mu) * rr, hl);
      STAMP(1);
      if (wave < 5) {
        v4u T[8];
        tr8(Lq, T);
        f32x4 acc[8];
#pragma unroll
        for (int i = 0; i < 8; ++i) acc[i] = mfma32(A, T[i], Z4);
        if (lane < 8 || (lane >= 24 && lane < 32)) {
          st_pairs(RED + ((wave * 2 + gq) * 2 + 0) * 64 + (lane & 7) * 8, acc, 0);
          st_pairs(RED + ((wave * 2 + gq) * 2 + 1) * 64 + (lane & 7) * 8, acc, 1);
        }
      }
      STAMP(2);
      tag = __builtin_amdgcn_readfirstlane((u32)epoch) + 1u;
      LBAR();
    }
    if (wave >= 6) {
      const int t = tid - 384;
      float s = 0.f;
#pragma unroll
      for (int k = 0; k < 10; ++k) s += RED[k * 128 + t];
      __hip_atomic_store((gu64*)(ws + OFF_QG) + g * 128 + t, ((u64)tag << 32) | __float_as_uint(s), RLX_AGENT);
      STAMP(3);
    }
    if (wave < 4) {
      const int b = tid >> 7, d = tid & 127;
      gu64* q0 = (gu64*)(ws + OFF_QG) + (h * 4 + (d >> 6)) * 128 + b * 64 + (d & 63);
      u64 a, c;
      for (int sp = 0;; ++sp) {
        a = __hip_atomic_load(q0, RLX_AGENT);
        c = __hip_atomic_load(q0 + 256, RLX_AGENT);
        const bool ok = ((u32)(a >> 32) == tag) & ((u32)(c >> 32) == tag);
        if (__all(ok) || sp > SPIN_CAP) break;
      }
      const float q = (__uint_as_float((u32)a) + __uint_as_float((u32)c)) * (0.08838834764831845f * 1.4426950408889634f);
      const u32 hi = pk2(q, 0.f) & 0xFFFFu;
      const u32 lo = pk2(q - bflo(hi), 0.f) & 0xFFFFu;
      QV[(0 * 2 + b) * 128 + d] = (u16)hi;
      QV[(1 * 2 + b) * 128 + d] = (u16)lo;
      STAMP(4);
    }
    LBAR();
    {
      f32x4 acc = Z4;
      const LAS u16* qb = QV + ((lane & 1) * 2 + ((lane >> 1) & 1)) * 128 + gq * 8;
#pragma unroll
      for (int s = 0; s < 4; ++s) acc = mfma32(wk[s], *(const LAS v4u*)(qb + s * 32), acc);
      f32x4 o;
#pragma unroll
      for (int i = 0; i < 4; ++i) o[i] = acc[i] + dpp<0xB1>(acc[i]);
      const int b = r16 >> 1;
      const int c0 = part * 128 + wave * 16 + gq * 4;
      const int off = (r16 == 0 || r16 == 2) ? OFF_QA + ((b * 32 + h) * 256 + (c0 >> 1)) * 8 : OOB;
      st_sc1(rs, off, v4u{pk2(o[0], o[1]), tag, pk2(o[2], o[3]), tag});
      STAMP(5);
    }
    const int fL = ((g >> 3) >> 2) * 8 + (g & 7);
    const bool isF = fL < 27;
    const int fs = fL * 4 + ((g >> 3) & 3);
    {
      const v4u sv = {SENT, SENT, SENT, SENT};
#pragma unroll
      for (int i = 0; i < 4; ++i) st_sc1(rs, OFF_IN + g * 32768 + i * 8192 + tid * 16, sv);
      int off = OOB;
      if (wave == 5) off = OFF_D + g * 1024 + lane * 16;
      if (wave == 6 && g < 20) off = OFF_E + g * 1024 + lane * 16;
      if (wave < 5 && isF) off = OFF_FIN + ((wave * 8 + (lane >> 3)) * 108 + fs) * 128 + (lane & 7) * 16;
      st_sc1(rs, off, sv);
      DRAIN();
      LBAR();
      if (tid == 0) __hip_atomic_store((gu64*)(ws + OFF_READY) + g, epoch + 1ull, RLX_AGENT);
      STAMP(6);
    }
    v2u Tv[8];
    v4u Tf[8];
    v2u T2[8];
    v4u xa;
    {
      v4u Lv[4], Lf[8], L2[4];
      {
        const rsrc_t rv = mk(p.wuv, 512 * 4096 * 2);
        const int off = (((part * 128 + wave * 16 + gq * 4) * 32 + h) * 128 + r16 * 8) * 2;
#pragma unroll
        for (int i = 0; i < 4; ++i) Lv[i] = ldw<2>(rv, off + i * 8192);
      }
      SB();
      if (FW_DELAY) while (rt() < t_start + FW_DELAY) __builtin_amdgcn_s_sleep(2);
      {
        const int cb = r16 & 3, ks = r16 >> 2;
        const bool act = isF && wave < 5;
        const u16* src = act ? ((cb & 2) ? p.w3 : p.w1) + (size_t)(wave * 128 + ks * 32 + gq * 8) * FF + fs * 16 + (cb & 1) * 8 : p.x;
        const int stride = act ? FF : 0;
#pragma unroll
        for (int i = 0; i < 8; ++i) {
          if (FW_AUX == 2) Lf[i] = __builtin_nontemporal_load((const GAS v4u*)(src + i * stride));
          else Lf[i] = *(const GAS v4u*)(src + i * stride);
        }
        const rsrc_t r2 = mk(p.w2, FF * MM * 2);
        const int off = ((isF && wave >= 3) ? 0 : OOB) + ((fs * 16 + gq * 4) * MM + (wave - 3) * 128 + r16 * 8) * 2;
#pragma unroll
        for (int i = 0; i < 4; ++i) L2[i] = ldw<2>(r2, off + i * (MM * 2));
        xa = ldw<0>(rx, (wave < 5 ? 0 : OOB) + (((r16 >> 1) & 1) * MM + wave * 128 + (r16 >> 2) * 32 + gq * 8) * 2);
      }
      SB();
      tr4(Lv, Tv);
      STAMP(7);
      tr8(Lf, Tf);
      tr4(L2, T2);
    }
    LAS float* CS = (LAS float*)(L + 32768);
    if (wave < 5) {
      const v4u ones = {0x3F803F80u, 0x3F803F80u, 0x3F803F80u, 0x3F803F80u};
      f32x4 a[8];
#pragma unroll
      for (int i = 0; i < 8; ++i) a[i] = mfma32(ones, Tf[i], Z4);
      if (lane < 16) {
        LAS f32x4* dst = (LAS f32x4*)(CS + (wave * 4 + (r16 >> 2)) * 32 + (r16 & 3) * 8);
        dst[0] = f32x4{a[0][0], a[1][0], a[2][0], a[3][0]};
        dst[1] = f32x4{a[4][0], a[5][0], a[6][0], a[7][0]};
      }
    }
    STAMP(8);
    LAS float* R1 = (LAS float*)(L + 8192);
    LAS float* ML = (LAS float*)(L + 6144);
    LAS float* R2 = (LAS float*)(L + 40960);
    {
      v4u sv[4];
      const int soff = OFF_ST + ((part * 128 + wave * 16 + gq * 4) * 32 + h) * 16;
      for (int sp = 0;; ++sp) {
        CBAR();
#pragma unroll
        for (int i = 0; i < 4; ++i) sv[i] = ld_sc1(rs, soff + i * 512);
        bool ok = true;
#pragma unroll
        for (int i = 0; i < 4; ++i) ok &= (sv[i].y == tag) & (sv[i].w == tag);
        if (__all(ok) || sp > SPIN_CAP) break;
        __builtin_amdgcn_s_sleep(1);
      }
      STAMP(9);
      v4u pv[4];
      const int ioff = OFF_IN + g * 32768 + (wave * 16 + gq * 4) * 256 + r16 * 16;
      CBAR();
#pragma unroll
      for (int i = 0; i < 4; ++i) pv[i] = ld_sc1(rs, ioff + i * 256);
      SB();
      float mw = fmaxf(fmaxf(__uint_as_float(sv[0].x), __uint_as_float(sv[1].x)), fmaxf(__uint_as_float(sv[2].x), __uint_as_float(sv[3].x)));
      mw = max32(max16(mw));
      float wi[4], lw = 0.f;
#pragma unroll
      for (int i = 0; i < 4; ++i) { wi[i] = __builtin_amdgcn_exp2f(__uint_as_float(sv[i].x) - mw); lw += wi[i] * __uint_as_float(sv[i].z); }
      lw = red16(lw, lw); lw = red32(lw, lw);
      if (lane == 0) *(LAS f32x2*)(ML + wave * 2) = f32x2{mw, lw};
      for (int sp = 0;; ++sp) {
        const bool bad = umax2(umax2(umax4(pv[0]), umax4(pv[1])), umax2(umax4(pv[2]), umax4(pv[3]))) == SENT;
        if (!__any(bad) || sp > SPIN_CAP) break;
        CBAR();
#pragma unroll
        for (int i = 0; i < 4; ++i) pv[i] = ld_sc1(rs, ioff + i * 256);
      }
      STAMP(10);
      float a[8];
#pragma unroll
      for (int dw = 0; dw < 4; ++dw) { a[2 * dw] = wi[0] * bflo(pv[0][dw]); a[2 * dw + 1] = wi[0] * bfhi(pv[0][dw]); }
#pragma unroll
      for (int i = 1; i < 4; ++i)
#pragma unroll
        for (int dw = 0; dw < 4; ++dw) { a[2 * dw] += wi[i] * bflo(pv[i][dw]); a[2 * dw + 1] += wi[i] * bfhi(pv[i][dw]); }
      {
        const float b0 = red32(a[0], a[4]), b1 = red32(a[1], a[5]), b2 = red32(a[2], a[6]), b3 = red32(a[3], a[7]);
        const float c0 = red16(b0, b2), c1 = red16(b1, b3);
        *(LAS f32x2*)(R1 + wave * 128 + r16 * 8 + gq * 2) = f32x2{c0, c1};
      }
      LBAR();
      STAMP(11);
      {
        const int b = r16 >> 3, pw = (r16 >> 1) & 3; const bool hl = r16 & 1;
        const f32x4 ml0 = *(LAS f32x4*)(ML + b * 8), ml1 = *(LAS f32x4*)(ML + b * 8 + 4);
        f32x4 s0 = *(const LAS f32x4*)(R1 + (r16 >> 1) * 128 + wave * 16 + gq * 4);
        const float M = fmaxf(fmaxf(ml0.x, ml0.z), fmaxf(ml1.x, ml1.z));
        const float e0 = __builtin_amdgcn_exp2f(ml0.x - M), e1 = __builtin_amdgcn_exp2f(ml0.z - M), e2 = __builtin_amdgcn_exp2f(ml1.x - M), e3 = __builtin_amdgcn_exp2f(ml1.z - M);
        const float den = (e0 * ml0.y + e1 * ml0.w) + (e2 * ml1.y + e3 * ml1.w);
        const float sc = ((pw & 2) ? ((pw & 1) ? e3 : e2) : ((pw & 1) ? e1 : e0)) * __builtin_amdgcn_rcpf(den);
        s0 *= sc;
        const v2u A = {split2(s0.x, s0.y, hl), split2(s0.z, s0.w, hl)};
        f32x4 acc[8];
#pragma unroll
        for (int i = 0; i < 8; ++i) acc[i] = mfma16(A, Tv[i], Z4);
        float o[8];
#pragma unroll
        for (int i = 0; i < 8; ++i) o[i] = (acc[i][0] + acc[i][1]) + (acc[i][2] + acc[i][3]);
        const f32x4 r = {red16(o[0], o[4]), red16(o[1], o[5]), red16(o[2], o[6]), red16(o[3], o[7])};
        *(LAS f32x4*)(R2 + (wave * 2 + (gq >> 1)) * 128 + r16 * 8 + (gq & 1) * 4) = r;
      }
      LBAR();
      if (wave == 0) {
        f32x4 s = Z4;
#pragma unroll
        for (int w = 0; w < 8; ++w) s += *(const LAS f32x4*)(R2 + w * 256 + lane * 4);
        st_sc1(rs, OFF_D + g * 1024 + lane * 16, nosent4(s));
      }
      STAMP(12);
    }
    if (isF) {
      LAS float* RF = (LAS float*)(L + 8192);
      LAS float* SS = (LAS float*)(L + 6144 + 256);
      LAS u16* HH = (LAS u16*)(L + 6144 + 512);
      if (wave < 5) {
        const int ks = r16 >> 2, b = (r16 >> 1) & 1; const bool hl = r16 & 1;
        const int eoff = OFF_E + (b * MM + wave * 128 + ks * 32 + gq * 8) * 4;
        v4u ev[8];
        for (int sp = 0;; ++sp) {
          CBAR();
#pragma unroll
          for (int k = 0; k < 4; ++k) { ev[2 * k] = ld_sc1(rs, eoff + k * 5120); ev[2 * k + 1] = ld_sc1(rs, eoff + k * 5120 + 16); }
          u32 mx = 0;
#pragma unroll
          for (int k = 0; k < 8; ++k) mx = umax2(mx, umax4(ev[k]));
          if (!__any(mx == SENT) || sp > SPIN_CAP) break;
          __builtin_amdgcn_s_sleep(1);
        }
        STAMP(13);
        f32x4 s0 = {bflo(xa.x), bfhi(xa.x), bflo(xa.y), bfhi(xa.y)}, s1 = {bflo(xa.z), bfhi(xa.z), bflo(xa.w), bfhi(xa.w)};
#pragma unroll
        for (int k = 0; k < 4; ++k) { s0 += __builtin_bit_cast(f32x4, ev[2 * k]); s1 += __builtin_bit_cast(f32x4, ev[2 * k + 1]); }
        const v4u A = {split2(s0.x, s0.y, hl), split2(s0.z, s0.w, hl), split2(s1.x, s1.y, hl), split2(s1.z, s1.w, hl)};
        f32x4 acc[8];
#pragma unroll
        for (int i = 0; i < 8; ++i) acc[i] = mfma32(A, Tf[i], Z4);
        float t1 = ((s0.x + s0.y) + (s0.z + s0.w)) + ((s1.x + s1.y) + (s1.z + s1.w));
        float t2 = ((s0.x * s0.x + s0.y * s0.y) + (s0.z * s0.z + s0.w * s0.w)) + ((s1.x * s1.x + s1.y * s1.y) + (s1.z * s1.z + s1.w * s1.w));
        float t = red32(t1, t2);
        t = red16(t, t);
        t += dpp<0x128>(t); t += dpp<0x124>(t);
        if ((lane & 29) == 0) SS[((lane >> 5) * 2 + ((lane >> 1) & 1)) * 8 + wave] = t;
        if (gq == ks) {
          st_pairs(RF + ((wave * 4 + gq) * 2 + 0) * 32 + (r16 & 3) * 8, acc, 0);
          st_pairs(RF + ((wave * 4 + gq) * 2 + 1) * 32 + (r16 & 3) * 8, acc, 1);
        }
      }
      float cs = 0.f;
      if (wave == 7) {
#pragma unroll
        for (int k = 0; k < 20; ++k) cs += CS[k * 32 + (lane & 31)];
      }
      LBAR();
      if (wave == 7) {
        const int b = lane >> 5, c = lane & 31;
        float s = 0.f;
#pragma unroll
        for (int k = 0; k < 20; ++k) s += RF[k * 64 + lane];
        const f32x4 a0 = *(LAS f32x4*)(SS + b * 8), a1 = *(LAS f32x4*)(SS + 16 + b * 8);
        const float t1 = ((a0.x + a0.y) + (a0.z + a0.w)) + SS[b * 8 + 4], t2 = ((a1.x + a1.y) + (a1.z + a1.w)) + SS[16 + b * 8 + 4];
        const float mean = t1 * (1.0f / MM);
        const float var = fmaxf(t2 * (1.0f / MM) - mean * mean, 0.f);
        const float rstd = __builtin_amdgcn_rsqf(var + 1e-5f);
        const float v = (s - mean * cs) * rstd;
        auto sw = __builtin_amdgcn_permlane16_swap(__float_as_uint(v), __float_as_uint(v), false, false);
        const float gt = __uint_as_float(sw[0]), up = __uint_as_float(sw[1]);
        const float hh = gt * __builtin_amdgcn_rcpf(1.0f + __builtin_amdgcn_exp2f(-1.4426950408889634f * gt)) * up;
        const u32 hi = pk2(hh, 0.f) & 0xFFFFu;
        const u32 lo = pk2(hh - bflo(hi), 0.f) & 0xFFFFu;
        HH[gq * 16 + r16] = (u16)((c < 16) ? hi : lo);
      }
      LBAR();
      STAMP(14);
      if (wave >= 3) {
        const v2u A = *(const LAS v2u*)(HH + (r16 & 3) * 16 + gq * 4);
        f32x4 acc[8];
#pragma unroll
        for (int i = 0; i < 8; ++i) acc[i] = mfma16(A, T2[i], Z4);
        const int b = gq >> 1, hf = gq & 1;
        f32x4 o;
#pragma unroll
        for (int i = 0; i < 4; ++i) {
          const float l0 = acc[i][0] + acc[i][1], l1 = acc[i][2] + acc[i][3];
          const float h0 = acc[4 + i][0] + acc[4 + i][1], h1 = acc[4 + i][2] + acc[4 + i][3];
          const float vl = b ? l1 : l0, vh = b ? h1 : h0;
          o[i] = hf ? vh : vl;
        }
        const int col = (wave - 3) * 128 + r16 * 8 + hf * 4;
        const int red = b * 20 + (col >> 5);
        st_sc1(rs, OFF_FIN + (red * 108 + fs) * 128 + (col & 31) * 4, nosent4(o));
        STAMP(15);
      }
    }
  } else {
    const int a = bid - 128;
    const int b = a >> 6;
    const u64 epoch = __hip_atomic_load((gu64*)(ws + OFF_EPOCH), RLX_AGENT);
    while (rt() < t_start + ATT_DELAY) __builtin_amdgcn_s_sleep(1);
    {
      const u16* base = p.ckv + (size_t)a * 128 * 512;
#pragma unroll
      for (int i = 0; i < 16; ++i) {
        const int row = wave * 16 + i;
        const int f = ((row & 3) << 2) | ((row >> 2) & 3);
        const u16* src = base + row * 512 + ((lane ^ f) * 8);
        unsigned keep;
        asm volatile("s_mov_b32 %0, m0\n\ts_mov_b32 m0, %2\n\ts_nop 0\n\tglobal_load_lds_dwordx4 %1, off nt\n\ts_mov_b32 m0, %0"
                     : "=&s"(keep) : "v"(src), "s"(row * 1024) : "memory");
      }
    }
    STAMP(1);
    asm volatile("s_barrier" ::: "memory");
    v4u qv[8];
    const int qoff = OFF_QA + b * 65536 + tid * 16;
#pragma unroll
    for (int i = 0; i < 8; ++i) qv[i] = ld_sc1(rs, qoff + i * 8192);
    SB();
    asm volatile("s_waitcnt vmcnt(8)\n\ts_barrier" ::: "memory");
    STAMP(2);
    const u32 tag = __builtin_amdgcn_readfirstlane((u32)epoch) + 1u;
    const int r32 = lane & 31, hf = lane >> 5;
    v4u fr[32];
    if (wave < 4) {
      const int row = wave * 32 + r32;
#pragma unroll
      for (int s = 0; s < 32; ++s) fr[s] = *(const LAS v4u*)(L + kvoff(row, 2 * s + hf));
    } else {
      const int u = wave - 4;
      const int blk = (lane >> 4) & 1, q = (lane >> 2) & 3, pp = lane & 3;
      const int rg = blk * 4 + pp;
      const int cbase = ((rg & 1) * 16 + (rg >> 1) * 4);
#pragma unroll
      for (int t = 0; t < 4; ++t)
#pragma unroll
        for (int ks = 0; ks < 8; ++ks) {
          const int c = u * 128 + t * 32 + cbase;
          const int row0 = ks * 16 + 4 * hf + q;
          const s16x4 lo = __builtin_amdgcn_ds_read_tr16_b64_v4i16((LAS s16x4*)(L + kvoff(row0, c >> 3) + (c & 4) * 2));
          const s16x4 hi = __builtin_amdgcn_ds_read_tr16_b64_v4i16((LAS s16x4*)(L + kvoff(row0 + 8, c >> 3) + (c & 4) * 2));
          const v2u l2 = __builtin_bit_cast(v2u, lo), h2 = __builtin_bit_cast(v2u, hi);
          fr[t * 8 + ks] = v4u{l2.x, l2.y, h2.x, h2.y};
        }
    }
    SB();
    STAMP(3);
    for (int sp = 0;; ++sp) {
      bool ok = true;
#pragma unroll
      for (int i = 0; i < 8; ++i) ok &= (qv[i].y == tag) & (qv[i].w == tag);
      if (__all(ok) || sp > SPIN_CAP) break;
      __builtin_amdgcn_s_sleep(8);
      CBAR();
#pragma unroll
      for (int i = 0; i < 8; ++i) qv[i] = ld_sc1(rs, qoff + i * 8192);
    }
    STAMP(4);
    LAS unsigned char* QA = L + 131072;
#pragma unroll
    for (int i = 0; i < 8; ++i) {
      const int gp = i * 512 + tid, hd = gp >> 7, c = (gp & 127) * 4;
      *(LAS v2u*)(QA + kvoff(hd, c >> 3) + (c & 4) * 2) = v2u{qv[i].x, qv[i].z};
    }
    LBAR();
    STAMP(5);
    LAS float* TM = (LAS float*)(L);
    LAS float* LS = (LAS float*)(L + 512);
    LAS v4u* PB = (LAS v4u*)(L + 2048);
    if (wave < 4) {
      f32x16 s;
#pragma unroll
      for (int i = 0; i < 16; ++i) s[i] = 0.f;
      v4u bq[8];
#pragma unroll
      for (int k = 0; k < 8; ++k) bq[k] = *(const LAS v4u*)(QA + kvoff(r32, 2 * k + hf));
      SB();
#pragma unroll
      for (int k = 0; k < 32; ++k) {
        s = mfmaB(fr[k], bq[k & 7], s);
        if (k + 8 < 32) bq[k & 7] = *(const LAS v4u*)(QA + kvoff(r32, 2 * (k + 8) + hf));
        SB();
      }
      float m = s[0];
#pragma unroll
      for (int i = 1; i < 16; ++i) m = fmaxf(m, s[i]);
      m = max32(m);
      if (lane < 32) TM[wave * 32 + lane] = m;
      STAMP(6);
      LBAR();
      m = fmaxf(fmaxf(TM[r32], TM[32 + r32]), fmaxf(TM[64 + r32], TM[96 + r32]));
      float l = 0.f;
#pragma unroll
      for (int i = 0; i < 16; ++i) { s[i] = __builtin_amdgcn_exp2f(s[i] - m); l += s[i]; }
      PB[(wave * 2 + 0) * 64 + lane] = v4u{pk2(s[0], s[1]), pk2(s[2], s[3]), pk2(s[4], s[5]), pk2(s[6], s[7])};
      PB[(wave * 2 + 1) * 64 + lane] = v4u{pk2(s[8], s[9]), pk2(s[10], s[11]), pk2(s[12], s[13]), pk2(s[14], s[15])};
      LS[wave * 64 + lane] = l;
      LBAR();
    } else {
      const int u = wave - 4;
      const u32 th = __builtin_amdgcn_readfirstlane((u32)((epoch + 1ull) >> 32));
      for (int sp = 0;; ++sp) {
        CBAR();
        const v4u rv = ld_sc1(rs, OFF_READY + lane * 16);
        const bool ok = (rv.x == tag) & (rv.y == th) & (rv.z == tag) & (rv.w == th);
        if (__all(ok) || sp > SPIN_CAP) break;
        __builtin_amdgcn_s_sleep(1);
      }
      STAMP(6);
      LBAR();
      LBAR();
      STAMP(7);
      v4u pf[8];
#pragma unroll
      for (int k = 0; k < 8; ++k) pf[k] = PB[k * 64 + lane];
      LAS unsigned char* OT = L + 16384 + u * 8704;
#pragma unroll
      for (int tp = 0; tp < 2; ++tp) {
        f32x16 o0, o1;
#pragma unroll
        for (int i = 0; i < 16; ++i) { o0[i] = 0.f; o1[i] = 0.f; }
#pragma unroll
        for (int k = 0; k < 8; ++k) { o0 = mfmaB(fr[tp * 16 + k], pf[k], o0); o1 = mfmaB(fr[tp * 16 + 8 + k], pf[k], o1); }
        LAS v4u* dst = (LAS v4u*)(OT + r32 * 272 + tp * 128 + hf * 32);
        dst[0] = v4u{nosent(pk2(o0[0], o0[1])), nosent(pk2(o0[2], o0[3])), nosent(pk2(o0[4], o0[5])), nosent(pk2(o0[6], o0[7]))};
        dst[1] = v4u{nosent(pk2(o0[8], o0[9])), nosent(pk2(o0[10], o0[11])), nosent(pk2(o0[12], o0[13])), nosent(pk2(o0[14], o0[15]))};
        dst[4] = v4u{nosent(pk2(o1[0], o1[1])), nosent(pk2(o1[2], o1[3])), nosent(pk2(o1[4], o1[5])), nosent(pk2(o1[6], o1[7]))};
        dst[5] = v4u{nosent(pk2(o1[8], o1[9])), nosent(pk2(o1[10], o1[11])), nosent(pk2(o1[12], o1[13])), nosent(pk2(o1[14], o1[15]))};
#pragma unroll
        for (int i = 0; i < 4; ++i) {
          const int hd = i * 8 + (lane >> 3);
          const v4u v = *(const LAS v4u*)(OT + hd * 272 + tp * 128 + (lane & 7) * 16);
          st_sc1(rs, OFF_IN + (hd * 4 + u) * 32768 + a * 256 + tp * 128 + (lane & 7) * 16, v);
        }
        if (tp == 0) STAMP(8);
      }
      {
        const float m = fmaxf(fmaxf(TM[r32], TM[32 + r32]), fmaxf(TM[64 + r32], TM[96 + r32]));
        float l = 0.f;
#pragma unroll
        for (int k = 0; k < 8; ++k) l += LS[k * 32 + r32];
        st_sc1(rs, lane < 32 ? OFF_ST + ((u * 128 + a) * 32 + lane) * 16 : OOB, v4u{__float_as_uint(m), tag, __float_as_uint(l), tag});
      }
      STAMP(9);
    }
    asm volatile("s_barrier" ::: "memory");
    const int ai = a >> 3;
    if (ai < 10) {
      const int pr = (ai >> 1) * 8 + (a & 7);
      const int ksl = pr / 10, ms = (pr % 10) * 2 + (ai & 1);
      const int hd = ksl * 8 + wave;
      const int cb = r16 & 3, ks = r16 >> 2;
      v4u T[8];
      {
        v4u Lo[8];
        const rsrc_t ro = mk(p.wo, 4096 * MM * 2);
        const int off = ((hd * 128 + ks * 32 + gq * 8) * MM + ms * 32 + cb * 8) * 2;
#pragma unroll
        for (int i = 0; i < 8; ++i) Lo[i] = ldw<WO_AUX>(ro, off + i * (MM * 2));
        SB();
        tr8(Lo, T);
      }
      STAMP(10);
      const int bb = (r16 >> 1) & 1; const bool hl = r16 & 1;
      const int doff = OFF_D + hd * 4096 + (bb * 128 + ks * 32 + gq * 8) * 4;
      v4u dv[8];
      for (int sp = 0;; ++sp) {
        CBAR();
#pragma unroll
        for (int k = 0; k < 4; ++k) { dv[2 * k] = ld_sc1(rs, doff + k * 1024); dv[2 * k + 1] = ld_sc1(rs, doff + k * 1024 + 16); }
        u32 mx = 0;
#pragma unroll
        for (int k = 0; k < 8; ++k) mx = umax2(mx, umax4(dv[k]));
        if (!__any(mx == SENT) || sp > SPIN_CAP) break;
        __builtin_amdgcn_s_sleep(1);
      }
      STAMP(11);
      f32x4 s0 = Z4, s1 = Z4;
#pragma unroll
      for (int k = 0; k < 4; ++k) { s0 += __builtin_bit_cast(f32x4, dv[2 * k]); s1 += __builtin_bit_cast(f32x4, dv[2 * k + 1]); }
      const v4u A = {split2(s0.x, s0.y, hl), split2(s0.z, s0.w, hl), split2(s1.x, s1.y, hl), split2(s1.z, s1.w, hl)};
      f32x4 acc[8];
#pragma unroll
      for (int i = 0; i < 8; ++i) acc[i] = mfma32(A, T[i], Z4);
      LAS float* RO = (LAS float*)(L + 65536);
      if (gq == ks) {
        st_pairs(RO + ((wave * 4 + gq) * 2 + 0) * 32 + cb * 8, acc, 0);
        st_pairs(RO + ((wave * 4 + gq) * 2 + 1) * 32 + cb * 8, acc, 1);
      }
      LBAR();
      if (wave == 4) {
        f32x4 s = Z4;
#pragma unroll
        for (int k = 0; k < 8; ++k) s += *(const LAS f32x4*)(RO + (gq * 8 + k) * 64 + r16 * 4);
#pragma unroll
        for (int i = 0; i < 4; ++i) { s[i] = red16(s[i], s[i]); s[i] = red32(s[i], s[i]); }
        st_sc1(rs, lane < 16 ? OFF_E + ((ksl * 2 + (r16 >> 3)) * MM + ms * 32 + (r16 & 7) * 4) * 4 : OOB, nosent4(s));
      }
      STAMP(12);
    } else if (ai < 15) {
      const int red = (ai - 10) * 8 + (a & 7);
      const int bb = red / 20, c0 = (red % 20) * 32;
      LAS float* RFN = (LAS float*)(L + 65536);
      const int c4 = tid & 7, srcs = tid >> 3;
      const u32 xw = ((const GAS u32*)(p.x + bb * MM + c0))[r16];
      v4u e4;
      const int eoff = OFF_E + (((lane >> 3) & 3) * 2 + bb) * (MM * 4) + (c0 + c4 * 4) * 4;
      for (int sp = 0;; ++sp) {
        CBAR();
        e4 = ld_sc1(rs, eoff);
        if (!__any(umax4(e4) == SENT) || sp > SPIN_CAP) break;
        __builtin_amdgcn_s_sleep(4);
      }
      STAMP(12);
      const int foff = OFF_FIN + (red * 108 + srcs) * 128 + c4 * 16;
      const int off2 = srcs < 44 ? foff + 64 * 128 : OOB;
      v4u f0, f1;
      for (int sp = 0;; ++sp) {
        CBAR();
        f0 = ld_sc1(rs, foff);
        f1 = ld_sc1(rs, off2);
        if (!__any(umax2(umax4(f0), umax4(f1)) == SENT) || sp > SPIN_CAP) break;
        __builtin_amdgcn_s_sleep(1);
      }
      STAMP(13);
      f32x4 s = __builtin_bit_cast(f32x4, f0) + __builtin_bit_cast(f32x4, f1);
      if (wave == 7 && lane < 32) s += __builtin_bit_cast(f32x4, e4);
      {
        const float b0 = red32(s[0], s[2]), b1 = red32(s[1], s[3]);
        float c = red16(b0, b1);
        c += dpp<0x128>(c);
        if (r16 < 8) RFN[wave * 32 + r16 * 4 + gq] = c;
      }
      LBAR();
      if (wave == 4 && lane < 16) {
        float o0 = bflo(xw), o1 = bfhi(xw);
#pragma unroll
        for (int w = 0; w < 8; ++w) { const f32x2 v = *(LAS f32x2*)(RFN + w * 32 + 2 * lane); o0 += v.x; o1 += v.y; }
        ((GAS u32*)(p.out + bb * MM + c0))[lane] = pk2(o0, o1);
        if (red == 0 && lane == 0) __hip_atomic_store((gu64*)(ws + OFF_EPOCH), epoch + 1ull, RLX_AGENT);
      }
      STAMP(14);
    }
  }
  STAMP_FLUSH();
}

extern "C" void kernel_launch(void* const* d_in, const int* in_sizes, int n_in, void* d_out, int out_size, void* d_ws, size_t ws_size,
                              hipStream_t stream) {
  for(int i=0;i<n_in;i++) fprintf(stderr,"in%d=%p ",i,d_in[i]); fprintf(stderr,"out=%p ws=%p\n",d_out,d_ws);
  { hipPointerAttribute_t at; if(hipPointerGetAttributes(&at,d_ws)==hipSuccess) fprintf(stderr,"ws type=%d flags=%u managed=%d\n",(int)at.type,at.allocationFlags,at.isManaged); }
  Params p;
  p.x = (const u16*)d_in[0]; p.ckv = (const u16*)d_in[1]; p.wq = (const u16*)d_in[2]; p.wuk = (const u16*)d_in[3];
  p.wuv = (const u16*)d_in[4]; p.wo = (const u16*)d_in[5]; p.w1 = (const u16*)d_in[7]; p.w3 = (const u16*)d_in[8]; p.w2 = (const u16*)d_in[9];
  p.out = (u16*)d_out; p.ws = (unsigned char*)d_ws;
  (void)hipFuncSetAttribute((const void*)mega, hipFuncAttributeMaxDynamicSharedMemorySize, 163840);
  mega<<<256, 512, 163840, stream>>>(p);
}
```

```cpp
#define C_MODE 2
#define C_WAIT 8
#define WK_SLEEP 8
#include <hip/hip_runtime.h>
#include <stdint.h>

typedef unsigned short u16; typedef unsigned int u32; typedef unsigned long long u64;
typedef u32 v4u __attribute__((ext_vector_type(4)));
typedef u32 v2u __attribute__((ext_vector_type(2)));
typedef float f32x2 __attribute__((ext_vector_type(2)));
typedef float f32x4 __attribute__((ext_vector_type(4)));
typedef float f32x16 __attribute__((ext_vector_type(16)));
typedef short s16x4 __attribute__((ext_vector_type(4)));
typedef __bf16 b16x8 __attribute__((ext_vector_type(8)));
typedef __bf16 b16x2 __attribute__((ext_vector_type(2)));
typedef __amdgpu_buffer_rsrc_t rsrc_t;
#define GAS __attribute__((address_space(1)))
#define LAS __attribute__((address_space(3)))
typedef GAS u64 gu64;
#define RLX_AGENT __ATOMIC_RELAXED, __HIP_MEMORY_SCOPE_AGENT
#define DI __device__ __forceinline__

#define ATT_DELAY 120
#define FW_DELAY 500
#define APOLL_SLEEP 0
#define WV_MODE 0
#define KV_MAP 3
#define WS_BASE 0
#define WO_DELAY 550
#define CODE_BYTES 21504
#define FW_AUX 2
#define WO_AUX 2

constexpr int MM = 640, FF = 1728;
constexpr int OFF_EPOCH = 0;
constexpr int OFF_READY = 1024;
constexpr int OFF_RDY0  = 2048;
constexpr int OFF_QG    = 4096;
constexpr int OFF_QA    = OFF_QG + 131072;
constexpr int OFF_ST    = OFF_QA + 131072;
constexpr int OFF_D     = OFF_ST + 262144;
constexpr int OFF_E     = OFF_D + 65536;
constexpr int OFF_FIN   = OFF_E + 10240;
constexpr int OFF_IN    = OFF_FIN + 40 * 54 * 128;
constexpr int WS_BYTES  = OFF_IN + 128 * 32768;
constexpr int OFF_STAMP = WS_BYTES;
constexpr u32 SENT = 0xFFFFFFFFu;
constexpr int SPIN_CAP = 1 << 16;
constexpr int OOB = 0x40000000;

DI u64 rt() { return __builtin_amdgcn_s_memrealtime(); }
#define STAMP(i) do {} while (0)
#define STAMP_FLUSH() do {} while (0)

DI rsrc_t mk(const void* p, int bytes) { return __builtin_amdgcn_make_buffer_rsrc((void*)p, 0, bytes, 0x00020000); }
template <int AUX> DI v4u ldw(rsrc_t rs, int off) { return __builtin_amdgcn_raw_buffer_load_b128(rs, off, 0, AUX); }
DI v4u ld_sc1(rsrc_t rs, int off) { return __builtin_amdgcn_raw_buffer_load_b128(rs, off, 0, 16); }
DI void st_sc1(rsrc_t rs, int off, v4u v) { __builtin_amdgcn_raw_buffer_store_b128(v, rs, off, 0, 16); }
#define CBAR() asm volatile("" ::: "memory")
#define LBAR() asm volatile("s_waitcnt lgkmcnt(0)\n\ts_barrier" ::: "memory")
#define DRAIN() asm volatile("s_waitcnt vmcnt(0)" ::: "memory")
#define SB() __builtin_amdgcn_sched_barrier(0)

DI float bflo(u32 w) { return __uint_as_float(w << 16); }
DI float bfhi(u32 w) { return __uint_as_float(w & 0xFFFF0000u); }
DI u32 pk2(float a, float b) { f32x2 v = {a, b}; return __builtin_bit_cast(u32, __builtin_convertvector(v, b16x2)); }
DI u32 umax4(v4u v) { u32 a = v.x > v.y ? v.x : v.y, b = v.z > v.w ? v.z : v.w; return a > b ? a : b; }
DI u32 umax2(u32 a, u32 b) { return a > b ? a : b; }
DI u32 nosent(u32 x) { return x; }
DI v4u nosent4(f32x4 v) { return v4u{nosent(__float_as_uint(v.x)), nosent(__float_as_uint(v.y)), nosent(__float_as_uint(v.z)), nosent(__float_as_uint(v.w))}; }
DI f32x4 mfma32(v4u a, v4u b, f32x4 c) { return __builtin_amdgcn_mfma_f32_16x16x32_bf16(__builtin_bit_cast(b16x8, a), __builtin_bit_cast(b16x8, b), c, 0, 0, 0); }
DI f32x4 mfma16(v2u a, v2u b, f32x4 c) { return __builtin_amdgcn_mfma_f32_16x16x16bf16_1k(__builtin_bit_cast(s16x4, a), __builtin_bit_cast(s16x4, b), c, 0, 0, 0); }
DI f32x16 mfmaB(v4u a, v4u b, f32x16 c) { return __builtin_amdgcn_mfma_f32_32x32x16_bf16(__builtin_bit_cast(b16x8, a), __builtin_bit_cast(b16x8, b), c, 0, 0, 0); }
DI void tr8(const v4u (&L)[8], v4u (&T)[8]) {
#pragma unroll
  for (int dw = 0; dw < 4; ++dw)
#pragma unroll
    for (int p = 0; p < 4; ++p) {
      T[2 * dw][p]     = __builtin_amdgcn_perm(L[2 * p + 1][dw], L[2 * p][dw], 0x05040100u);
      T[2 * dw + 1][p] = __builtin_amdgcn_perm(L[2 * p + 1][dw], L[2 * p][dw], 0x07060302u);
    }
}
DI void tr4(const v4u (&L)[4], v2u (&T)[8]) {
#pragma unroll
  for (int dw = 0; dw < 4; ++dw)
#pragma unroll
    for (int p = 0; p < 2; ++p) {
      T[2 * dw][p]     = __builtin_amdgcn_perm(L[2 * p + 1][dw], L[2 * p][dw], 0x05040100u);
      T[2 * dw + 1][p] = __builtin_amdgcn_perm(L[2 * p + 1][dw], L[2 * p][dw], 0x07060302u);
    }
}
DI u32 split2(float a, float b, bool lo) {
  u32 h = pk2(a, b);
  u32 l = pk2(a - bflo(h), b - bfhi(h));
  return lo ? l : h;
}
template <int CTRL> DI float dpp(float v) { return __builtin_bit_cast(float, __builtin_amdgcn_update_dpp(0, __builtin_bit_cast(int, v), CTRL, 0xf, 0xf, true)); }
DI float row_sum16(float v) { v += dpp<0xB1>(v); v += dpp<0x4E>(v); v += dpp<0x141>(v); v += dpp<0x140>(v); return v; }
DI float red32(float a, float b) { auto r = __builtin_amdgcn_permlane32_swap(__float_as_uint(a), __float_as_uint(b), false, false); return __uint_as_float(r[0]) + __uint_as_float(r[1]); }
DI float red16(float a, float b) { auto r = __builtin_amdgcn_permlane16_swap(__float_as_uint(a), __float_as_uint(b), false, false); return __uint_as_float(r[0]) + __uint_as_float(r[1]); }
DI float max32(float a) { auto r = __builtin_amdgcn_permlane32_swap(__float_as_uint(a), __float_as_uint(a), false, false); return fmaxf(__uint_as_float(r[0]), __uint_as_float(r[1])); }
DI float max16(float a) { auto r = __builtin_amdgcn_permlane16_swap(__float_as_uint(a), __float_as_uint(a), false, false); return fmaxf(__uint_as_float(r[0]), __uint_as_float(r[1])); }
DI float rdl(float v, int l) { return __builtin_bit_cast(float, __builtin_amdgcn_readlane(__builtin_bit_cast(int, v), l)); }
DI int kvoff(int row, int ch) { return row * 1024 + 16 * (ch ^ (((row & 3) << 2) | ((row >> 2) & 3))); }
DI void st_pairs(LAS float* dst, const f32x4 (&acc)[8], int q) {
  ((LAS f32x4*)dst)[0] = f32x4{acc[0][2 * q] + acc[0][2 * q + 1], acc[1][2 * q] + acc[1][2 * q + 1], acc[2][2 * q] + acc[2][2 * q + 1], acc[3][2 * q] + acc[3][2 * q + 1]};
  ((LAS f32x4*)dst)[1] = f32x4{acc[4][2 * q] + acc[4][2 * q + 1], acc[5][2 * q] + acc[5][2 * q + 1], acc[6][2 * q] + acc[6][2 * q + 1], acc[7][2 * q] + acc[7][2 * q + 1]};
}

DI int off256(int row, int ch) { return row * 256 + 16 * (ch ^ (((row & 3) << 2) | ((row >> 2) & 3))); }
template <bool NT> DI void dma16(const void* sbase, u32 voff, int ldsoff) {
  unsigned keep;
  if (NT) asm volatile("s_mov_b32 %0, m0\n\ts_mov_b32 m0, %3\n\ts_nop 0\n\tglobal_load_lds_dwordx4 %1, %2 nt\n\ts_mov_b32 m0, %0"
               : "=&s"(keep) : "v"(voff), "s"(sbase), "s"(ldsoff) : "memory");
  else asm volatile("s_mov_b32 %0, m0\n\ts_mov_b32 m0, %3\n\ts_nop 0\n\tglobal_load_lds_dwordx4 %1, %2\n\ts_mov_b32 m0, %0"
               : "=&s"(keep) : "v"(voff), "s"(sbase), "s"(ldsoff) : "memory");
}
DI v2u trr(LAS unsigned char* p) { return __builtin_bit_cast(v2u, __builtin_amdgcn_ds_read_tr16_b64_v4i16((LAS s16x4*)p)); }
#define PIN4(x) asm volatile("" : "+v"(x))
DI float sum4(f32x4 a) { return (a[0] + a[1]) + (a[2] + a[3]); }

struct Params {
  const u16 *x, *ckv, *wq, *wuk, *wuv, *wo, *w1, *w3, *w2;
  u16* out;
  unsigned char* ws;
};

__global__ void __launch_bounds__(512, 2) mega(Params p) {
  extern __shared__ __attribute__((aligned(16))) unsigned char smem[];
  LAS unsigned char* L = (LAS unsigned char*)smem;
  const int bid = blockIdx.x, tid = threadIdx.x, lane = tid & 63;
  const int wave = __builtin_amdgcn_readfirstlane(tid >> 6);
  {
    const u32 voff = (u32)tid * 64u < (u32)(CODE_BYTES - 64) ? (u32)tid * 64u : (u32)(CODE_BYTES - 64);
    u64 pc0; u32 keep;
    asm volatile("s_cmp_lt_u32 %3, 128\n\ts_cbranch_scc1 1f\n\ts_getpc_b64 %0\n\ts_mov_b32 %1, m0\n\ts_mov_b32 m0, 0x20000\n\ts_nop 0\n\t"
                 "global_load_lds_dword %2, %0\n\ts_mov_b32 m0, %1\n1:" : "=&s"(pc0), "=&s"(keep) : "v"(voff), "s"(bid) : "memory", "scc");
  }
  unsigned char* ws = p.ws;
  const rsrc_t rs = mk(ws, WS_BYTES);
  const u64 t_start = rt();
  int stampv = 0;
  STAMP(0);
  u64 epoch;
  const int r16 = lane & 15, gq = lane >> 4;
  const f32x4 Z4 = {0.f, 0.f, 0.f, 0.f};

  if (__builtin_expect(bid < 128, 1)) {
    const int g = bid, gi = g >> 3, h = (g & 7) + 8 * (gi >> 2), part = gi & 3;
    LAS unsigned char* WQ = L;
    LAS unsigned char* WK = L + 40960;
    LAS unsigned char* QV = L + 73728;
    const rsrc_t rx = mk(p.x, 2560);
    u32 tag, th;
    {
      LAS unsigned char* XL = L + 77824;
      LAS float* LNS = (LAS float*)(L + 76800);
      if (wave == 7) { dma16<false>(p.x, lane * 16, 77824); dma16<false>(p.x, 1024 + lane * 16, 77824 + 1024); dma16<false>(p.x, 2048 + (lane & 31) * 16, 77824 + 2048); }
      {
        const u32 vo = (u32)(part * 160 + wave * 20 + gq) * 8192u + h * 256;
#pragma unroll
        for (int i = 0; i < 5; ++i) dma16<true>(p.wq, vo + i * 32768 + ((r16 ^ ((gq << 2) | ((wave * 5 + i) & 3))) * 16), (wave * 5 + i) * 1024);
      }
      asm volatile("s_barrier" ::: "memory");
      if (wave == 7) {
        asm volatile("s_waitcnt vmcnt(5) lgkmcnt(0)" ::: "memory");
        v2u xs[5];
#pragma unroll
        for (int i = 0; i < 5; ++i) xs[i] = *(LAS v2u*)(XL + (lane >> 5) * 1280 + (lane & 31) * 8 + i * 256);
        float s = 0.f;
#pragma unroll
        for (int i = 0; i < 5; ++i) s += (bflo(xs[i].x) + bfhi(xs[i].x)) + (bflo(xs[i].y) + bfhi(xs[i].y));
        s = row_sum16(s); s = red16(s, s);
        const float mean = s * (1.0f / MM);
        float q = 0.f;
#pragma unroll
        for (int i = 0; i < 5; ++i) {
          const float a = bflo(xs[i].x) - mean, b = bfhi(xs[i].x) - mean, c = bflo(xs[i].y) - mean, d = bfhi(xs[i].y) - mean;
          q += (a * a + b * b) + (c * c + d * d);
        }
        q = row_sum16(q); q = red16(q, q);
        const float rstd = (0.08838834764831845f * 1.4426950408889634f) / sqrtf(q * (1.0f / MM) + 1e-5f);
        if ((lane & 31) == 0) *(LAS f32x2*)(LNS + (lane >> 5) * 2) = f32x2{mean, rstd};
        if (lane < 20) *(LAS v4u*)(XL + 2560 + lane * 16) = v4u{0x3F803F80u, 0x3F803F80u, 0x3F803F80u, 0x3F803F80u};
      }
      {
        const u32 vo = (u32)(part * 128 + wave * 16 + gq) * 8192u + h * 256;
#pragma unroll
        for (int i = 0; i < 4; ++i) dma16<true>(p.wuk, vo + i * 32768 + ((r16 ^ ((gq << 2) | i)) * 16), 40960 + (wave * 4 + i) * 1024);
      }
      const int q4 = (lane >> 2) & 3, pp = lane & 3;
      int tra = off256(gq * 8 + q4, wave * 2 + (pp >> 1)) + (pp & 1) * 8;
      int trb = off256(gq * 8 + q4 + 4, wave * 2 + (pp >> 1)) + (pp & 1) * 8;
      int qoff = (r16 < 2) ? OFF_QG + (((h * 4 + part) * 2 + r16) * 64 + wave * 8 + gq * 2) * 8 : OOB;
      int xo = 77824 + ((lane & 2) ? 2560 : ((lane & 1) * MM + part * 160) * 2) + gq * 16;
      PIN4(tra); PIN4(trb); PIN4(qoff); PIN4(xo);
      STAMP(1);
      asm volatile("s_load_dwordx2 %0, %1, 0x0\n\ts_waitcnt lgkmcnt(0)" : "=s"(epoch) : "s"(ws) : "memory");
      tag = (u32)epoch + 1u; th = (u32)((epoch + 1ull) >> 32);
      asm volatile("s_waitcnt vmcnt(4)" ::: "memory");
      LBAR();
      STAMP(2);
      v4u A[5], xb[5];
#pragma unroll
      for (int i = 0; i < 5; ++i) {
        const v2u l2 = trr(WQ + tra + i * 8192), h2 = trr(WQ + trb + i * 8192); A[i] = v4u{l2.x, l2.y, h2.x, h2.y};
        xb[i] = *(const LAS v4u*)(L + xo + i * 64);
      }
      const f32x2 ln = *(LAS f32x2*)(LNS + (lane & 1) * 2);
      f32x4 acc = Z4;
#pragma unroll
      for (int i = 0; i < 5; ++i) acc = mfma32(A[i], xb[i], acc);
      f32x4 o;
#pragma unroll
      for (int i = 0; i < 4; ++i) o[i] = (acc[i] - ln.x * dpp<0x4E>(acc[i])) * ln.y;
      st_sc1(rs, qoff, v4u{pk2(o[0], o[1]), tag, pk2(o[2], o[3]), tag});
      STAMP(3);
    }
    v4u wk[4];
    asm volatile("s_waitcnt vmcnt(1)" ::: "memory");
#pragma unroll
    for (int s = 0; s < 4; ++s) { wk[s] = *(const LAS v4u*)(WK + off256(wave * 16 + r16, s * 4 + gq)); PIN4(wk[s]); }
    if (wave < 4) {
      const int goff = OFF_QG + h * 4096 + tid * 16;
      v4u v;
      for (int sp = 0;; ++sp) {
        CBAR();
        v = ld_sc1(rs, goff);
        const bool ok = (v.y == tag) & (v.w == tag);
        if (__all(ok) || sp > SPIN_CAP) break;
        if (APOLL_SLEEP) __builtin_amdgcn_s_sleep(APOLL_SLEEP);
      }
      const int pb = tid >> 5;
      *(LAS v2u*)(QV + ((pb & 1) * 4 + (pb >> 1)) * 272 + (tid & 31) * 8) = v2u{v.x, v.z};
      STAMP(4);
    } else if (wave == 4) {
      for (int sp = 0;; ++sp) {
        CBAR();
        const v4u rv = ld_sc1(rs, OFF_READY + lane * 16);
        const bool ok = (rv.x == tag) & (rv.y == th) & (rv.z == tag) & (rv.w == th);
        if (__all(ok) || sp > SPIN_CAP) break;
        __builtin_amdgcn_s_sleep(2);
      }
    }
    LBAR();
    {
      f32x4 acc = Z4;
      const LAS unsigned char* qb = QV + (lane & 7) * 272 + gq * 16;
#pragma unroll
      for (int s = 0; s < 4; ++s) acc = mfma32(wk[s], *(const LAS v4u*)(qb + s * 64), acc);
      f32x4 o;
#pragma unroll
      for (int i = 0; i < 4; ++i) { o[i] = acc[i] + dpp<0xB1>(acc[i]); o[i] += dpp<0x4E>(o[i]); }
      const int c0 = part * 128 + wave * 16 + gq * 4;
      const int off = (r16 == 0 || r16 == 4) ? OFF_QA + (((r16 >> 2) * 32 + h) * 512 + c0) * 2 : OOB;
      __builtin_amdgcn_raw_buffer_store_b64(v2u{pk2(o[0], o[1]), pk2(o[2], o[3])}, rs, off, 0, 16);
      STAMP(5);
    }
    LBAR();
    const bool isO = gi < 5;
    const int oi = gi * 8 + (g & 7), ksl = oi / 10, ms = oi % 10;
    const int fL = ((gi - 5) >> 1) * 8 + (g & 7);
    const bool isF = gi >= 5 && gi < 13 && fL < 27;
    const int fs = fL * 2 + ((gi - 5) & 1);
    if (isO) {
      while (rt() < t_start + WO_DELAY) __builtin_amdgcn_s_sleep(2);
      const int hd = ksl * 8 + wave;
      const int cb = r16 & 7, ks = r16 >> 3;
      v4u T0[8], T1[8];
      {
        v4u La[8], Lb[8];
        const rsrc_t ro = mk(p.wo, 4096 * MM * 2);
        const int off = ((hd * 128 + ks * 32 + gq * 8) * MM + ms * 64 + cb * 8) * 2;
#pragma unroll
        for (int i = 0; i < 8; ++i) La[i] = ldw<WO_AUX>(ro, off + i * (MM * 2));
#pragma unroll
        for (int i = 0; i < 8; ++i) Lb[i] = ldw<WO_AUX>(ro, off + (64 + i) * (MM * 2));
        SB();
        tr8(La, T0); tr8(Lb, T1);
#pragma unroll
        for (int i = 0; i < 8; ++i) { PIN4(T0[i]); PIN4(T1[i]); }
      }
      const u32 xw = __builtin_amdgcn_raw_buffer_load_b32(rx, ksl == 0 ? ((lane >> 5) * MM + ms * 64 + (lane & 31) * 2) * 2 : OOB, 0, 0);
      STAMP(7);
      const int doff = OFF_D + (hd * 4 + (r16 & 3)) * 512 + ((r16 >> 2) & 1) * 256 + (ks * 32 + gq * 8) * 2;
      LAS float* RO = (LAS float*)(L);
      LAS float* rdst = RO + ((wave * 2 + ks) * 2 + (gq & 1)) * 64 + cb * 8;
      v4u d0, d1;
      for (int sp = 0;; ++sp) {
        CBAR();
        d0 = ld_sc1(rs, doff); d1 = ld_sc1(rs, doff + 128);
        if (!__any(umax2(umax4(d0), umax4(d1)) == SENT) || sp > SPIN_CAP) break;
        __builtin_amdgcn_s_sleep(1);
      }
      STAMP(8);
      f32x4 acc[8];
#pragma unroll
      for (int i = 0; i < 8; ++i) acc[i] = mfma32(d0, T0[i], Z4);
#pragma unroll
      for (int i = 0; i < 8; ++i) acc[i] = mfma32(d1, T1[i], acc[i]);
      if ((gq >> 1) == ks) {
        ((LAS f32x4*)rdst)[0] = f32x4{sum4(acc[0]), sum4(acc[1]), sum4(acc[2]), sum4(acc[3])};
        ((LAS f32x4*)rdst)[1] = f32x4{sum4(acc[4]), sum4(acc[5]), sum4(acc[6]), sum4(acc[7])};
      }
      LBAR();
      STAMP(9);
      if (wave == 0) {
        f32x2 s = {bflo(xw), bfhi(xw)};
#pragma unroll
        for (int k = 0; k < 16; ++k) s += *(const LAS f32x2*)(RO + k * 128 + lane * 2);
        __builtin_amdgcn_raw_buffer_store_b32(nosent(pk2(s.x, s.y)), rs, OFF_E + ((ksl * 2 + (lane >> 5)) * MM + ms * 64 + (lane & 31) * 2) * 2, 0, 16);
      }
      STAMP(10);
    } else if (isF) {
      if (FW_DELAY) while (rt() < t_start + FW_DELAY) __builtin_amdgcn_s_sleep(2);
      const int cb = r16 & 7, ks = r16 >> 3;
      v4u T0[8], T1[8], T2[8];
      {
        v4u La[8], Lb[8], Lc[8];
        const u16* src = ((cb & 4) ? p.w3 : p.w1) + (size_t)(wave * 64 + ks * 32 + gq * 8) * FF + fs * 32 + (cb & 3) * 8;
#pragma unroll
        for (int i = 0; i < 8; ++i) La[i] = __builtin_nontemporal_load((const GAS v4u*)(src + i * FF));
        const u16* src2 = wave < 2 ? src + (size_t)512 * FF : p.x;
        const int st2 = wave < 2 ? FF : 0;
#pragma unroll
        for (int i = 0; i < 8; ++i) Lb[i] = __builtin_nontemporal_load((const GAS v4u*)(src2 + i * st2));
        const rsrc_t r2 = mk(p.w2, FF * MM * 2);
        const int off = (wave >= 3 ? 0 : OOB) + ((fs * 32 + gq * 8) * MM + (wave - 3) * 128 + r16 * 8) * 2;
#pragma unroll
        for (int i = 0; i < 8; ++i) Lc[i] = ldw<2>(r2, off + i * (MM * 2));
        SB();
        tr8(La, T0); tr8(Lb, T1); tr8(Lc, T2);
#pragma unroll
        for (int i = 0; i < 8; ++i) { PIN4(T0[i]); PIN4(T1[i]); PIN4(T2[i]); }
      }
      LAS float* CS = (LAS float*)(L + 16384);
      LAS float* RF = (LAS float*)(L);
      LAS float* SS = (LAS float*)(L + 8192);
      LAS u16* HH = (LAS u16*)(L + 8192 + 512);
      const v4u ones = {0x3F803F80u, 0x3F803F80u, 0x3F803F80u, 0x3F803F80u};
      {
        f32x4 a[8];
#pragma unroll
        for (int i = 0; i < 8; ++i) a[i] = mfma32(ones, T0[i], Z4);
        if (wave < 2) {
#pragma unroll
          for (int i = 0; i < 8; ++i) a[i] = mfma32(ones, T1[i], a[i]);
        }
        if (lane < 16) {
          LAS f32x4* dst = (LAS f32x4*)(CS + (wave * 2 + ks) * 64 + cb * 8);
          dst[0] = f32x4{a[0][0], a[1][0], a[2][0], a[3][0]};
          dst[1] = f32x4{a[4][0], a[5][0], a[6][0], a[7][0]};
        }
      }
      LBAR();
      float cs = 0.f;
#pragma unroll
      for (int k = 0; k < 16; ++k) cs += CS[k * 64 + lane];
      PIN4(cs);
      STAMP(7);
      const int eoff = OFF_E + ((r16 & 3) * 2 + ((r16 >> 2) & 1)) * (MM * 2) + (wave * 64 + ks * 32 + gq * 8) * 2;
      const int eoff2 = wave < 2 ? eoff + 1024 : OOB;
      LAS float* rdst = RF + ((wave * 2 + ks) * 2 + (gq & 1)) * 64 + cb * 8;
      v4u e0, e1;
      for (int sp = 0;; ++sp) {
        CBAR();
        e0 = ld_sc1(rs, eoff); e1 = ld_sc1(rs, eoff2);
        if (!__any(umax2(umax4(e0), umax4(e1)) == SENT) || sp > SPIN_CAP) break;
        __builtin_amdgcn_s_sleep(1);
      }
      STAMP(11);
      f32x4 gr = mfma32(e0, e0, Z4), sm = mfma32(e0, ones, Z4);
      f32x4 acc[8];
#pragma unroll
      for (int i = 0; i < 8; ++i) acc[i] = mfma32(e0, T0[i], Z4);
      if (wave < 2) {
        gr = mfma32(e1, e1, gr); sm = mfma32(e1, ones, sm);
#pragma unroll
        for (int i = 0; i < 8; ++i) acc[i] = mfma32(e1, T1[i], acc[i]);
      }
      {
        float t2 = sum4(gr); t2 += dpp<0xB1>(t2); t2 += dpp<0x4E>(t2);
        const float t1 = sum4(sm);
        if (r16 == gq * 4) {
          SS[(gq & 1) * 16 + wave * 2 + (gq >> 1)] = t1;
          SS[32 + (gq & 1) * 16 + wave * 2 + (gq >> 1)] = t2;
        }
      }
      if ((gq >> 1) == ks) {
        ((LAS f32x4*)rdst)[0] = f32x4{sum4(acc[0]), sum4(acc[1]), sum4(acc[2]), sum4(acc[3])};
        ((LAS f32x4*)rdst)[1] = f32x4{sum4(acc[4]), sum4(acc[5]), sum4(acc[6]), sum4(acc[7])};
      }
      LBAR();
      STAMP(12);
      if (wave < 2) {
        const int b = wave;
        float s = 0.f;
#pragma unroll
        for (int k = 0; k < 16; ++k) s += RF[(k * 2 + b) * 64 + lane];
        float t = SS[(lane >> 5) * 32 + b * 16 + r16];
        t = row_sum16(t);
        const float t1 = rdl(t, 0), t2 = rdl(t, 32);
        const float mean = t1 * (1.0f / MM);
        const float var = fmaxf(t2 * (1.0f / MM) - mean * mean, 0.f);
        const float rstd = __builtin_amdgcn_rsqf(var + 1e-5f);
        const float v = (s - mean * cs) * rstd;
        auto sw = __builtin_amdgcn_permlane32_swap(__float_as_uint(v), __float_as_uint(v), false, false);
        const float gt = __uint_as_float(sw[0]), up = __uint_as_float(sw[1]);
        const float hh = gt * __builtin_amdgcn_rcpf(1.0f + __builtin_amdgcn_exp2f(-1.4426950408889634f * gt)) * up;
        const u32 hi = pk2(hh, 0.f) & 0xFFFFu;
        const u32 lo = pk2(hh - bflo(hi), 0.f) & 0xFFFFu;
        HH[b * 64 + lane] = (u16)(lane < 32 ? hi : lo);
      }
      LBAR();
      STAMP(13);
      if (wave >= 3) {
        const v4u A = *(const LAS v4u*)(HH + (r16 & 3) * 32 + gq * 8);
#pragma unroll
        for (int i = 0; i < 8; ++i) acc[i] = mfma32(A, T2[i], Z4);
        const int b = gq >> 1, hf = gq & 1;
        f32x4 o;
#pragma unroll
        for (int i = 0; i < 4; ++i) {
          const float l0 = acc[i][0] + acc[i][1], l1 = acc[i][2] + acc[i][3];
          const float h0 = acc[4 + i][0] + acc[4 + i][1], h1 = acc[4 + i][2] + acc[4 + i][3];
          const float vl = b ? l1 : l0, vh = b ? h1 : h0;
          o[i] = hf ? vh : vl;
        }
        const int col = (wave - 3) * 128 + r16 * 8 + hf * 4;
        const int red = b * 20 + (col >> 5);
        st_sc1(rs, OFF_FIN + (red * 54 + fs) * 128 + (col & 31) * 4, nosent4(o));
        STAMP(14);
      }
    }
  } else {
    const int a = bid - 128;
    const int b = a >> 6;
    {
      const v4u sv = {SENT, SENT, SENT, SENT};
      if (C_MODE == 0) {
#pragma unroll
        for (int i = 0; i < 4; ++i) st_sc1(rs, OFF_IN + a * 32768 + i * 8192 + tid * 16, sv);
      }
      int off = OOB;
      if (a < 40 && tid < 54 * 8) off = OFF_FIN + a * (54 * 128) + tid * 16;
      if (wave == 7) off = (lane < 32 ? OFF_D : OFF_QA - 512) + a * 512 + lane * 16;
      if (a >= 40 && a < 50 && wave == 0) off = OFF_E + (a - 40) * 1024 + lane * 16;
      st_sc1(rs, off, sv);
    }
    asm volatile("s_load_dwordx2 %0, %1, 0x0\n\ts_waitcnt lgkmcnt(0) vmcnt(0)\n\ts_barrier" : "=s"(epoch) : "s"(ws) : "memory");
    const u32 tag = (u32)epoch + 1u, th = (u32)((epoch + 1ull) >> 32);
    if (wave == 7) __builtin_amdgcn_raw_buffer_store_b64(v2u{tag, th}, rs, lane == 0 ? OFF_READY + a * 8 : OOB, 0, 16);
    STAMP(16);
    while (rt() < t_start + ATT_DELAY) __builtin_amdgcn_s_sleep(1);
    const int r32 = lane & 31, hf = lane >> 5;
    const u32 kvo = (u32)(b * 8192 + wave * 64 + (a & 63)) * 1024u; constexpr u32 KT = 2048 * 1024, KI = 256 * 1024;
#define KVD(t, i) dma16<true>(p.ckv, kvo + (t) * KT + (i) * KI + ((lane ^ ((((i) & 3) << 2) | ((2 * wave + ((i) >> 2)) & 3))) * 16), ((t) * 32 + wave * 8 + (i)) * 1024)
#define KVDMA(t) KVD(t, 0); KVD(t, 1); KVD(t, 2); KVD(t, 3); KVD(t, 4); KVD(t, 5); KVD(t, 6); KVD(t, 7)
#define WAITV(n) asm volatile("s_waitcnt vmcnt(" #n ")" ::: "memory")
#define BAR() asm volatile("s_barrier" ::: "memory")
    v4u fr[32];
    v4u qv[4];
    const u32 qoff = OFF_QA + b * 32768 + tid * 16;
    int ab[8];
    if (wave < 4) {
#pragma unroll
      for (int k = 0; k < 8; ++k) ab[k] = kvoff(r32, 2 * k + hf);
    } else {
      const int blk = (lane >> 4) & 1, q = (lane >> 2) & 3, pp = lane & 3;
      const int rg = blk * 4 + pp;
      const int cbase = ((rg & 1) * 16 + (rg >> 1) * 4);
#pragma unroll
      for (int t = 0; t < 4; ++t) {
        const int c = (wave - 4) * 128 + t * 32 + cbase;
        ab[2 * t] = kvoff(4 * hf + q, c >> 3) + (c & 4) * 2;
        ab[2 * t + 1] = kvoff(4 * hf + q + 8, c >> 3) + (c & 4) * 2;
      }
    }
#pragma unroll
    for (int k = 0; k < 8; ++k) PIN4(ab[k]);
#define VFRAGS(t) \
    _Pragma("unroll") for (int tt = 0; tt < 4; ++tt) _Pragma("unroll") for (int k2 = 0; k2 < 2; ++k2) { \
        const v2u l2 = trr(L + (t) * 32768 + k2 * 16384 + ab[2 * tt]), h2 = trr(L + (t) * 32768 + k2 * 16384 + ab[2 * tt + 1]); \
        fr[tt * 8 + (t) * 2 + k2] = v4u{l2.x, l2.y, h2.x, h2.y}; }
#define LDQ(i) asm volatile("global_load_dwordx4 %0, %1, %2 sc1" : "=v"(qv[i]) : "v"(qoff + (i) * 8192), "s"(ws) : "memory")
    const int vh = a >> 2, vj = a & 3;
#define WVDMA(w, i) dma16<true>(p.wuv, (u32)(vj * 128 + (w) * 16 + (i) * 4 + gq) * 8192u + vh * 256 + ((r16 ^ ((gq << 2) | (i))) * 16), ((w) * 4 + (i)) * 1024)
    if (wave < 4) {
      v4u rv;
      const u32 ro = OFF_READY + lane * 16;
      KVDMA(0); KVDMA(1);
      asm volatile("s_cmp_lg_u32 %3, 0\n\ts_cbranch_scc1 1f\n\tglobal_load_dwordx4 %0, %1, %2 sc1\n1:" : "=v"(rv) : "v"(ro), "s"(ws), "s"(wave) : "memory", "scc");
      KVDMA(2);
      WAITV(16); BAR();
      STAMP(1);
      KVDMA(3);
      asm volatile("s_waitcnt vmcnt(16)" : "+v"(rv) :: "memory");
      if (wave == 0) for (int sp = 0;; ++sp) {
        const bool ok = (rv.x == tag) & (rv.y == th) & (rv.z == tag) & (rv.w == th);
        if (__all(ok) || sp > SPIN_CAP) break;
        asm volatile("global_load_dwordx4 %0, %1, %2 sc1\n\ts_waitcnt vmcnt(0)" : "=&v"(rv) : "v"(ro), "s"(ws) : "memory");
      }
      BAR();
      LDQ(0); LDQ(1); LDQ(2); LDQ(3);
      const int tb = wave * 32768;
      if (wave < 2) { _Pragma("unroll") for (int s = 0; s < 32; ++s) fr[s] = *(const LAS v4u*)(L + tb + ab[s & 7] + (s >> 3) * 256); }
      WAITV(12); BAR();
      STAMP(17);
      if (wave == 2) { _Pragma("unroll") for (int s = 0; s < 32; ++s) fr[s] = *(const LAS v4u*)(L + tb + ab[s & 7] + (s >> 3) * 256); }
      WAITV(4); BAR();
      STAMP(2);
      if (wave == 3) { _Pragma("unroll") for (int s = 0; s < 32; ++s) fr[s] = *(const LAS v4u*)(L + tb + ab[s & 7] + (s >> 3) * 256); }
    } else {
      BAR(); VFRAGS(0);
      STAMP(1);
      BAR();
      LDQ(0); LDQ(1); LDQ(2); LDQ(3);
      VFRAGS(1);
      BAR(); VFRAGS(2);
      STAMP(17);
      BAR();
      STAMP(2);
      VFRAGS(3);
    }
    SB();
    STAMP(3);
    LAS unsigned char* QA = L + 131072;
    asm volatile("s_waitcnt vmcnt(0)" : "+v"(qv[0]), "+v"(qv[1]), "+v"(qv[2]), "+v"(qv[3]) :: "memory");
    for (int sp = 0;; ++sp) {
      u32 mx = 0;
#pragma unroll
      for (int i = 0; i < 4; ++i) {
        mx = umax2(mx, umax4(qv[i]));
        *(LAS v4u*)(QA + kvoff(i * 8 + wave, lane)) = qv[i];
      }
      if (!__any(mx == SENT) || sp > SPIN_CAP) break;
      LDQ(0); LDQ(1); LDQ(2); LDQ(3);
      asm volatile("s_waitcnt vmcnt(0)" : "+v"(qv[0]), "+v"(qv[1]), "+v"(qv[2]), "+v"(qv[3]) :: "memory");
    }
    STAMP(4);
    LBAR();
    STAMP(5);
    LAS float* TM = (LAS float*)(L + 32768);
    LAS float* LS = (LAS float*)(L + 32768 + 512);
    LAS v4u* PB = (LAS v4u*)(L + 32768 + 2048);
    LAS unsigned char* WV = L;
    if (wave < 4) {
      f32x16 s;
#pragma unroll
      for (int i = 0; i < 16; ++i) s[i] = 0.f;
      v4u bq[8];
#pragma unroll
      for (int k = 0; k < 8; ++k) bq[k] = *(const LAS v4u*)(QA + ab[k]);
      SB();
#pragma unroll
      for (int k = 0; k < 32; ++k) {
        s = mfmaB(fr[k], bq[k & 7], s);
        if (k + 8 < 32) bq[k & 7] = *(const LAS v4u*)(QA + ab[k & 7] + ((k + 8) >> 3) * 256);
        SB();
      }
      float m = fmaxf(fmaxf(fmaxf(fmaxf(s[0], s[1]), s[2]), fmaxf(fmaxf(s[3], s[4]), s[5])), fmaxf(fmaxf(fmaxf(s[6], s[7]), s[8]), fmaxf(fmaxf(s[9], s[10]), s[11])));
      m = fmaxf(m, fmaxf(fmaxf(fmaxf(s[12], s[13]), s[14]), s[15]));
      m = max32(m);
      TM[r32 * 4 + wave] = m;
      STAMP(6);
      LBAR();
      const f32x4 tm = *(LAS f32x4*)(TM + r32 * 4);
      m = fmaxf(fmaxf(tm.x, tm.y), fmaxf(tm.z, tm.w));
#pragma unroll
      for (int i = 0; i < 16; ++i) s[i] = __builtin_amdgcn_exp2f(s[i] - m);
      PB[(wave * 2 + 0) * 64 + lane] = v4u{pk2(s[0], s[1]), pk2(s[2], s[3]), pk2(s[4], s[5]), pk2(s[6], s[7])};
      PB[(wave * 2 + 1) * 64 + lane] = v4u{pk2(s[8], s[9]), pk2(s[10], s[11]), pk2(s[12], s[13]), pk2(s[14], s[15])};
      LBAR();
      float l = 0.f;
#pragma unroll
      for (int i = 0; i < 16; ++i) l += s[i];
      LS[r32 * 8 + wave * 2 + hf] = l;
      LBAR();
    } else {
      const int u = wave - 4;
      LAS unsigned char* OT = L + 49152 + u * 8704;
      int ow = 49152 + u * 8704 + r32 * 272 + hf * 32, orr = 49152 + u * 8704 + (lane >> 3) * 272 + (lane & 7) * 16;
      int so = OFF_IN + ((lane >> 3) * 4 + u) * 32768 + a * 256 + (lane & 7) * 16;
      PIN4(ow); PIN4(orr); PIN4(so); (void)OT;
      if (C_MODE == 2) {
        const v4u sv = {SENT, SENT, SENT, SENT};
#pragma unroll
        for (int i = 0; i < 4; ++i) { st_sc1(rs, so + i * (32 * 32768), sv); st_sc1(rs, so + 128 + i * (32 * 32768), sv); }
      }
      WVDMA(u * 2, 0); WVDMA(u * 2, 1); WVDMA(u * 2, 2); WVDMA(u * 2, 3); WVDMA(u * 2 + 1, 0); WVDMA(u * 2 + 1, 1); WVDMA(u * 2 + 1, 2); WVDMA(u * 2 + 1, 3);
      int sto = lane < 32 ? OFF_ST + (lane * 4 + u) * 2048 + a * 16 : OOB;
      PIN4(sto);
      STAMP(6);
      LBAR();
      LBAR();
      STAMP(7);
      v4u pf[8];
#pragma unroll
      for (int k = 0; k < 8; ++k) pf[k] = PB[k * 64 + lane];
      f32x16 o0, o1, o2, o3;
#pragma unroll
      for (int i = 0; i < 16; ++i) { o0[i] = 0.f; o1[i] = 0.f; o2[i] = 0.f; o3[i] = 0.f; }
#define PK8(o, q) v4u{pk2(o[8 * (q)], o[8 * (q) + 1]), pk2(o[8 * (q) + 2], o[8 * (q) + 3]), pk2(o[8 * (q) + 4], o[8 * (q) + 5]), pk2(o[8 * (q) + 6], o[8 * (q) + 7])}
#pragma unroll
      for (int k = 0; k < 8; ++k) { o0 = mfmaB(fr[k], pf[k], o0); o1 = mfmaB(fr[8 + k], pf[k], o1); }
      SB();
      BAR();
      const f32x4 tm = *(LAS f32x4*)(TM + r32 * 4), l0 = *(LAS f32x4*)(LS + r32 * 8), l1 = *(LAS f32x4*)(LS + r32 * 8 + 4);
      v4u tv[4];
#pragma unroll
      for (int k = 0; k < 8; ++k) {
        o2 = mfmaB(fr[16 + k], pf[k], o2);
        o3 = mfmaB(fr[24 + k], pf[k], o3);
        if (k == 1) { *(LAS v4u*)(L + ow) = PK8(o0, 0); *(LAS v4u*)(L + ow + 16) = PK8(o0, 1); }
        if (k == 2) { *(LAS v4u*)(L + ow + 64) = PK8(o1, 0); *(LAS v4u*)(L + ow + 80) = PK8(o1, 1); }
        if (k == 3) { _Pragma("unroll") for (int i = 0; i < 4; ++i) tv[i] = *(const LAS v4u*)(L + orr + i * 2176); }
        if (k == 4 && C_MODE == 2) {
          WAITV(8);
          st_sc1(rs, sto, v4u{__float_as_uint(fmaxf(fmaxf(tm.x, tm.y), fmaxf(tm.z, tm.w))), tag, __float_as_uint(sum4(l0) + sum4(l1)), tag});
        }
        if (k == 5) { _Pragma("unroll") for (int i = 0; i < 4; ++i) st_sc1(rs, so + i * (32 * 32768), tv[i]); }
        SB();
      }
      STAMP(8);
      *(LAS v4u*)(L + ow + 128) = PK8(o2, 0); *(LAS v4u*)(L + ow + 144) = PK8(o2, 1);
      *(LAS v4u*)(L + ow + 192) = PK8(o3, 0); *(LAS v4u*)(L + ow + 208) = PK8(o3, 1);
#pragma unroll
      for (int i = 0; i < 4; ++i) tv[i] = *(const LAS v4u*)(L + orr + 128 + i * 2176);
#pragma unroll
      for (int i = 0; i < 4; ++i) st_sc1(rs, so + 128 + i * (32 * 32768), tv[i]);
      STAMP(18);
      if (C_MODE == 1) {
        v4u sv = {__float_as_uint(fmaxf(fmaxf(tm.x, tm.y), fmaxf(tm.z, tm.w))), tag, __float_as_uint(sum4(l0) + sum4(l1)), tag};
        PIN4(sv);
        DRAIN();
        st_sc1(rs, sto, sv);
      } else WAITV(9);
      STAMP(9);
    }
    asm volatile("s_barrier" ::: "memory");
    {
      LAS float* ML = (LAS float*)(L + 98304);
      LAS unsigned char* R1 = L + 98560;
      v4u Av[4];
      {
        const int q4 = (lane >> 2) & 3, pp = lane & 3;
#pragma unroll
        for (int s = 0; s < 4; ++s) {
          const int row = s * 32 + gq * 8 + q4;
          const v2u l2 = trr(WV + off256(row, wave * 2 + (pp >> 1)) + (pp & 1) * 8), h2 = trr(WV + off256(row + 4, wave * 2 + (pp >> 1)) + (pp & 1) * 8);
          Av[s] = v4u{l2.x, l2.y, h2.x, h2.y};
          PIN4(Av[s]);
        }
      }
      v4u sv;
      const int soff = OFF_ST + a * 2048 + (wave * 16 + gq * 4 + (lane & 3)) * 16;
      const int ioff = OFF_IN + a * 32768 + (wave * 16 + gq * 4) * 256 + r16 * 16;
      for (int sp = 0;; ++sp) {
        CBAR();
        sv = ld_sc1(rs, soff);
        const bool ok = (sv.y == tag) & (sv.w == tag);
        if (__all(ok) || sp > SPIN_CAP) break;
        __builtin_amdgcn_s_sleep(1);
      }
      if (C_WAIT) __builtin_amdgcn_s_sleep(C_WAIT);
      v4u pv[4];
      CBAR();
#pragma unroll
      for (int i = 0; i < 4; ++i) pv[i] = ld_sc1(rs, ioff + i * 256);
      SB();
      STAMP(10);
      const float ms = __uint_as_float(sv.x);
      float mw = fmaxf(ms, dpp<0xB1>(ms)); mw = fmaxf(mw, dpp<0x4E>(mw));
      mw = max32(max16(mw));
      const float wsrc = __builtin_amdgcn_exp2f(ms - mw);
      float lw = wsrc * __uint_as_float(sv.z);
      lw += dpp<0xB1>(lw); lw += dpp<0x4E>(lw);
      lw = red16(lw, lw); lw = red32(lw, lw);
      if (lane == 0) *(LAS f32x2*)(ML + wave * 2) = f32x2{mw, lw};
      LBAR();
      const f32x4 ml0 = *(LAS f32x4*)(ML + (wave >> 2) * 8), ml1 = *(LAS f32x4*)(ML + (wave >> 2) * 8 + 4);
      const float M = fmaxf(fmaxf(ml0.x, ml0.z), fmaxf(ml1.x, ml1.z));
      const float den = (__builtin_amdgcn_exp2f(ml0.x - M) * ml0.y + __builtin_amdgcn_exp2f(ml0.z - M) * ml0.w) + (__builtin_amdgcn_exp2f(ml1.x - M) * ml1.y + __builtin_amdgcn_exp2f(ml1.z - M) * ml1.w);
      const float wn = wsrc * __builtin_amdgcn_exp2f(mw - M) * __builtin_amdgcn_rcpf(den);
      v2u Aw = {pk2(dpp<0x00>(wn), dpp<0x55>(wn)), pk2(dpp<0xAA>(wn), dpp<0xFF>(wn))};
      PIN4(Aw);
      if (C_MODE != 1) for (int sp = 0;; ++sp) {
        const bool bad = umax2(umax2(umax4(pv[0]), umax4(pv[1])), umax2(umax4(pv[2]), umax4(pv[3]))) == SENT;
        if (!__any(bad) || sp > SPIN_CAP) break;
        CBAR();
#pragma unroll
        for (int i = 0; i < 4; ++i) pv[i] = ld_sc1(rs, ioff + i * 256);
      }
      STAMP(11);
      v2u T[8];
      tr4(pv, T);
      f32x4 c[8];
#pragma unroll
      for (int i = 0; i < 8; ++i) c[i] = mfma16(Aw, T[i], Z4);
      if (lane < 16) *(LAS v4u*)(R1 + wave * 272 + r16 * 16) = v4u{pk2(c[0][0], c[1][0]), pk2(c[2][0], c[3][0]), pk2(c[4][0], c[5][0]), pk2(c[6][0], c[7][0])};
      LBAR();
      f32x4 acc = Z4;
      const LAS unsigned char* rb = R1 + (lane & 7) * 272 + gq * 16;
#pragma unroll
      for (int s = 0; s < 4; ++s) acc = mfma32(Av[s], *(const LAS v4u*)(rb + s * 64), acc);
      f32x4 o;
#pragma unroll
      for (int i = 0; i < 4; ++i) { o[i] = acc[i] + dpp<0xB1>(acc[i]); o[i] += dpp<0x4E>(o[i]); }
      const int off = (r16 == 0 || r16 == 4) ? OFF_D + a * 512 + (r16 >> 2) * 256 + (wave * 16 + gq * 4) * 2 : OOB;
      __builtin_amdgcn_raw_buffer_store_b64(v2u{nosent(pk2(o[0], o[1])), nosent(pk2(o[2], o[3]))}, rs, off, 0, 16);
      STAMP(12);
    }
    if (a < 40) {
      const int red = a;
      const int bb = red / 20, c0 = (red % 20) * 32;
      LAS float* RFN = (LAS float*)(L + 32768);
      const int c4 = tid & 7, srcs = tid >> 3;
      u32 ew;
      const int eoff = OFF_E + ((gq * 2 + bb) * MM + c0 + r16 * 2) * 2;
      for (int sp = 0;; ++sp) {
        CBAR();
        ew = __builtin_amdgcn_raw_buffer_load_b32(rs, eoff, 0, 16);
        if (!__any(ew == SENT) || sp > SPIN_CAP) break;
        __builtin_amdgcn_s_sleep(4);
      }
      float h0 = bflo(ew), h1 = bfhi(ew);
      h0 = red16(h0, h0); h1 = red16(h1, h1); h0 = red32(h0, h0); h1 = red32(h1, h1);
      PIN4(h0); PIN4(h1);
      STAMP(13);
      const int foff = srcs < 54 ? OFF_FIN + (red * 54 + srcs) * 128 + c4 * 16 : OOB;
      v4u f0;
      for (int sp = 0;; ++sp) {
        CBAR();
        f0 = ld_sc1(rs, foff);
        if (!__any(umax4(f0) == SENT) || sp > SPIN_CAP) break;
        __builtin_amdgcn_s_sleep(1);
      }
      STAMP(14);
      const f32x4 s = __builtin_bit_cast(f32x4, f0);
      {
        const float b0 = red32(s[0], s[2]), b1 = red32(s[1], s[3]);
        float c = red16(b0, b1);
        c += dpp<0x128>(c);
        if (r16 < 8) RFN[wave * 32 + r16 * 4 + gq] = c;
      }
      LBAR();
      if (wave == 4 && lane < 16) {
#pragma unroll
        for (int w = 0; w < 7; ++w) { const f32x2 v = *(LAS f32x2*)(RFN + w * 32 + 2 * lane); h0 += v.x; h1 += v.y; }
        ((GAS u32*)(p.out + bb * MM + c0))[lane] = pk2(h0, h1);
        if (red == 0 && lane == 0) __hip_atomic_store((gu64*)(ws + OFF_EPOCH), epoch + 1ull, RLX_AGENT);
      }
      STAMP(15);
    }
  }
  STAMP_FLUSH();
}

extern "C" void kernel_launch(void* const* d_in, const int* in_sizes, int n_in, void* d_out, int out_size, void* d_ws, size_t ws_size,
                              hipStream_t stream) {
  Params p;
  p.x = (const u16*)d_in[0]; p.ckv = (const u16*)d_in[1]; p.wq = (const u16*)d_in[2]; p.wuk = (const u16*)d_in[3];
  p.wuv = (const u16*)d_in[4]; p.wo = (const u16*)d_in[5]; p.w1 = (const u16*)d_in[7]; p.w3 = (const u16*)d_in[8]; p.w2 = (const u16*)d_in[9];
  p.out = (u16*)d_out; p.ws = (unsigned char*)d_ws + WS_BASE;
  (void)hipFuncSetAttribute((const void*)mega, hipFuncAttributeMaxDynamicSharedMemorySize, 163840);
  mega<<<256, 512, 163840, stream>>>(p);
}
```

```cpp
#include <hip/hip_runtime.h>
#include <stdint.h>

typedef unsigned short u16; typedef unsigned int u32; typedef unsigned long long u64;
typedef u32 v4u __attribute__((ext_vector_type(4)));
typedef u32 v2u __attribute__((ext_vector_type(2)));
typedef float f32x2 __attribute__((ext_vector_type(2)));
typedef float f32x4 __attribute__((ext_vector_type(4)));
typedef float f32x16 __attribute__((ext_vector_type(16)));
typedef short s16x4 __attribute__((ext_vector_type(4)));
typedef __bf16 b16x8 __attribute__((ext_vector_type(8)));
typedef __bf16 b16x2 __attribute__((ext_vector_type(2)));
typedef __amdgpu_buffer_rsrc_t rsrc_t;
#define GAS __attribute__((address_space(1)))
#define LAS __attribute__((address_space(3)))
#define DI __device__ __forceinline__

#define ATT_DELAY 120
#define FW_DELAY 500
#define WO_DELAY 550
#define C_WAIT 4
#define ST_K 4
#define KV_ROT 0
#define CODE_BYTES 23808

constexpr int MM = 640, FF = 1728;
constexpr int OFF_EPOCH = 0;
constexpr int OFF_READY = 1024;
constexpr int OFF_QG    = 4096;
constexpr int OFF_QA    = OFF_QG + 131072;
constexpr int OFF_ST    = OFF_QA + 65536;
constexpr int OFF_D     = OFF_ST + 262144;
constexpr int OFF_E     = OFF_D + 65536;
constexpr int OFF_FIN   = OFF_E + 10240;
constexpr int OFF_IN    = OFF_FIN + 40 * 54 * 128;
constexpr int WS_BYTES  = OFF_IN + 128 * 32768;
constexpr int OFF_STAMP = WS_BYTES;
constexpr u32 SENT = 0xFFFFFFFFu;
constexpr int SPIN_CAP = 1 << 16;
constexpr int OOB = 0x40000000;

DI u64 rt() { return __builtin_amdgcn_s_memrealtime(); }
#define STAMP(i) do {} while (0)
#define STAMP_FLUSH() do {} while (0)

DI rsrc_t mk(const void* p, int bytes) { return __builtin_amdgcn_make_buffer_rsrc((void*)p, 0, bytes, 0x00020000); }
DI v4u ld_nt(rsrc_t rs, int off) { return __builtin_amdgcn_raw_buffer_load_b128(rs, off, 0, 2); }
DI v4u ld_sc1(rsrc_t rs, int off) { return __builtin_amdgcn_raw_buffer_load_b128(rs, off, 0, 16); }
DI void st_sc1(rsrc_t rs, int off, v4u v) { __builtin_amdgcn_raw_buffer_store_b128(v, rs, off, 0, 16); }
DI void st_sc1(rsrc_t rs, int off, v2u v) { __builtin_amdgcn_raw_buffer_store_b64(v, rs, off, 0, 16); }
#define CBAR() asm volatile("" ::: "memory")
#define BAR() asm volatile("s_barrier" ::: "memory")
#define LBAR() asm volatile("s_waitcnt lgkmcnt(0)\n\ts_barrier" ::: "memory")
#define WAITV(n) asm volatile("s_waitcnt vmcnt(" #n ")" ::: "memory")
#define SB() __builtin_amdgcn_sched_barrier(0)
#define PIN(x) asm volatile("" : "+v"(x))
#define LD_EPOCH(e) asm volatile("s_load_dwordx2 %0, %1, 0x0\n\ts_waitcnt lgkmcnt(0)" : "=s"(e) : "s"(ws) : "memory")

DI float bflo(u32 w) { return __uint_as_float(w << 16); }
DI float bfhi(u32 w) { return __uint_as_float(w & 0xFFFF0000u); }
DI u32 pk2(float a, float b) { f32x2 v = {a, b}; return __builtin_bit_cast(u32, __builtin_convertvector(v, b16x2)); }
DI u32 umax2(u32 a, u32 b) { return a > b ? a : b; }
DI u32 umax4(v4u v) { return umax2(umax2(v.x, v.y), umax2(v.z, v.w)); }
DI f32x4 mfma32(v4u a, v4u b, f32x4 c) { return __builtin_amdgcn_mfma_f32_16x16x32_bf16(__builtin_bit_cast(b16x8, a), __builtin_bit_cast(b16x8, b), c, 0, 0, 0); }
DI f32x16 mfmaB(v4u a, v4u b, f32x16 c) { return __builtin_amdgcn_mfma_f32_32x32x16_bf16(__builtin_bit_cast(b16x8, a), __builtin_bit_cast(b16x8, b), c, 0, 0, 0); }
DI void tr8(const v4u (&L)[8], v4u (&T)[8]) {
#pragma unroll
  for (int dw = 0; dw < 4; ++dw)
#pragma unroll
    for (int p = 0; p < 4; ++p) {
      T[2 * dw][p]     = __builtin_amdgcn_perm(L[2 * p + 1][dw], L[2 * p][dw], 0x05040100u);
      T[2 * dw + 1][p] = __builtin_amdgcn_perm(L[2 * p + 1][dw], L[2 * p][dw], 0x07060302u);
    }
}
template <int CTRL> DI float dpp(float v) { return __builtin_bit_cast(float, __builtin_amdgcn_update_dpp(0, __builtin_bit_cast(int, v), CTRL, 0xf, 0xf, true)); }
DI float row_sum16(float v) { v += dpp<0xB1>(v); v += dpp<0x4E>(v); v += dpp<0x141>(v); v += dpp<0x140>(v); return v; }
DI float red32(float a, float b) { auto r = __builtin_amdgcn_permlane32_swap(__float_as_uint(a), __float_as_uint(b), false, false); return __uint_as_float(r[0]) + __uint_as_float(r[1]); }
DI float red16(float a, float b) { auto r = __builtin_amdgcn_permlane16_swap(__float_as_uint(a), __float_as_uint(b), false, false); return __uint_as_float(r[0]) + __uint_as_float(r[1]); }
DI float max32(float a) { auto r = __builtin_amdgcn_permlane32_swap(__float_as_uint(a), __float_as_uint(a), false, false); return fmaxf(__uint_as_float(r[0]), __uint_as_float(r[1])); }
DI float max16(float a) { auto r = __builtin_amdgcn_permlane16_swap(__float_as_uint(a), __float_as_uint(a), false, false); return fmaxf(__uint_as_float(r[0]), __uint_as_float(r[1])); }
DI float rdl(float v, int l) { return __builtin_bit_cast(float, __builtin_amdgcn_readlane(__builtin_bit_cast(int, v), l)); }
DI int swz(int row) { return ((row & 3) << 2) | ((row >> 2) & 3); }
DI int kvoff(int row, int ch) { return row * 1024 + 16 * (ch ^ swz(row)); }
DI int off256(int row, int ch) { return row * 256 + 16 * (ch ^ swz(row)); }
template <bool NT> DI void dma16(const void* sbase, u32 voff, int ldsoff) {
  unsigned keep;
  if (NT) asm volatile("s_mov_b32 %0, m0\n\ts_mov_b32 m0, %3\n\ts_nop 0\n\tglobal_load_lds_dwordx4 %1, %2 nt\n\ts_mov_b32 m0, %0"
               : "=&s"(keep) : "v"(voff), "s"(sbase), "s"(ldsoff) : "memory");
  else asm volatile("s_mov_b32 %0, m0\n\ts_mov_b32 m0, %3\n\ts_nop 0\n\tglobal_load_lds_dwordx4 %1, %2\n\ts_mov_b32 m0, %0"
               : "=&s"(keep) : "v"(voff), "s"(sbase), "s"(ldsoff) : "memory");
}
DI v2u trr(LAS unsigned char* p) { return __builtin_bit_cast(v2u, __builtin_amdgcn_ds_read_tr16_b64_v4i16((LAS s16x4*)p)); }
DI float sum4(f32x4 a) { return (a[0] + a[1]) + (a[2] + a[3]); }
DI float sel(bool c, float t, float f) { asm("" : "+v"(t)); return c ? t : f; }
DI f32x4 red_rows(const f32x4 (&acc)[8], bool mks) {
  float r[8];
#pragma unroll
  for (int i = 0; i < 8; ++i) r[i] = red32(sel(mks, acc[i][2], acc[i][0]), sel(mks, acc[i][3], acc[i][1]));
  f32x4 o;
#pragma unroll
  for (int i = 0; i < 4; ++i) { const float v = red16(r[i], r[i + 4]); o[i] = v + dpp<0x128>(v); }
  return o;
}

struct Params {
  const u16 *x, *ckv, *wq, *wuk, *wuv, *wo, *w1, *w3, *w2;
  u16* out;
  unsigned char* ws;
};

__global__ void __launch_bounds__(512, 2) mega(Params p) {
  extern __shared__ __attribute__((aligned(16))) unsigned char smem[];
  LAS unsigned char* L = (LAS unsigned char*)smem;
  const int bid = blockIdx.x, tid = threadIdx.x, lane = tid & 63;
  const int wave = __builtin_amdgcn_readfirstlane(tid >> 6);
  {
    const u32 voff = (u32)tid * 64u < (u32)(CODE_BYTES - 64) ? (u32)tid * 64u : (u32)(CODE_BYTES - 64);
    const int doit = __builtin_amdgcn_readfirstlane((bid >> 7) & (bid >> 6) & ((wave * 4096 - CODE_BYTES) >> 31));
    u64 pc0; u32 keep;
    asm volatile("s_cmp_eq_u32 %3, 0\n\ts_cbranch_scc1 1f\n\ts_getpc_b64 %0\n\ts_mov_b32 %1, m0\n\ts_mov_b32 m0, 0x20000\n\ts_nop 0\n\t"
                 "global_load_lds_dword %2, %0\n\ts_mov_b32 m0, %1\n1:" : "=&s"(pc0), "=&s"(keep) : "v"(voff), "s"(doit) : "memory", "scc");
  }
  unsigned char* ws = p.ws;
  const rsrc_t rs = mk(ws, WS_BYTES);
  const u64 t_start = rt();
  int stampv = 0; (void)stampv;
  STAMP(0);
  u64 epoch;
  const int r16 = lane & 15, gq = lane >> 4;
  const f32x4 Z4 = {0.f, 0.f, 0.f, 0.f};
  const v4u SV = {SENT, SENT, SENT, SENT};

  if (__builtin_expect(bid < 128, 1)) {
    const int g = bid, gx = g & 7, gi = g >> 3, h = gx + 8 * (gi >> 2), part = gi & 3;
    constexpr int WQ = 0, WK = 40960, QV = 73728, LNS = 76800, XL = 77824;
    if (wave == 7) { dma16<false>(p.x, lane * 16, XL); dma16<false>(p.x, 1024 + lane * 16, XL + 1024); dma16<false>(p.x, 2048 + (lane & 31) * 16, XL + 2048); }
    {
      const u32 vo = (u32)(part * 160 + wave * 20 + gq) * 8192u + h * 256;
#pragma unroll
      for (int i = 0; i < 5; ++i) dma16<true>(p.wq, vo + i * 32768 + ((r16 ^ ((gq << 2) | ((wave * 5 + i) & 3))) * 16), WQ + (wave * 5 + i) * 1024);
    }
    BAR();
    if (wave == 7) {
      WAITV(5);
      v2u xs[5];
#pragma unroll
      for (int i = 0; i < 5; ++i) xs[i] = *(LAS v2u*)(L + XL + (lane >> 5) * 1280 + (lane & 31) * 8 + i * 256);
      float s = 0.f;
#pragma unroll
      for (int i = 0; i < 5; ++i) s += (bflo(xs[i].x) + bfhi(xs[i].x)) + (bflo(xs[i].y) + bfhi(xs[i].y));
      s = row_sum16(s); s = red16(s, s);
      const float mean = s * (1.0f / MM);
      float q = 0.f;
#pragma unroll
      for (int i = 0; i < 5; ++i) {
        const float a = bflo(xs[i].x) - mean, b = bfhi(xs[i].x) - mean, c = bflo(xs[i].y) - mean, d = bfhi(xs[i].y) - mean;
        q += (a * a + b * b) + (c * c + d * d);
      }
      q = row_sum16(q); q = red16(q, q);
      const float rstd = (0.08838834764831845f * 1.4426950408889634f) / sqrtf(q * (1.0f / MM) + 1e-5f);
      if ((lane & 31) == 0) *(LAS f32x2*)(L + LNS + (lane >> 5) * 8) = f32x2{mean, rstd};
      if (lane < 20) *(LAS v4u*)(L + XL + 2560 + lane * 16) = v4u{0x3F803F80u, 0x3F803F80u, 0x3F803F80u, 0x3F803F80u};
    }
    {
      const u32 vo = (u32)(part * 128 + wave * 16 + gq) * 8192u + h * 256;
#pragma unroll
      for (int i = 0; i < 4; ++i) dma16<true>(p.wuk, vo + i * 32768 + ((r16 ^ ((gq << 2) | i)) * 16), WK + (wave * 4 + i) * 1024);
    }
    const int q4 = (lane >> 2) & 3, pp = lane & 3;
    int tra = WQ + off256(gq * 8 + q4, wave * 2 + (pp >> 1)) + (pp & 1) * 8;
    int trb = WQ + off256(gq * 8 + q4 + 4, wave * 2 + (pp >> 1)) + (pp & 1) * 8;
    int qgo = (r16 < 2) ? OFF_QG + (h * 4 + part) * 1024 + wave * 128 + r16 * 64 + gq * 16 : OOB;
    int xo = XL + ((lane & 2) ? 2560 : ((lane & 1) * MM + part * 160) * 2) + gq * 16;
    int lno = LNS + (lane & 1) * 8;
    PIN(tra); PIN(trb); PIN(qgo); PIN(xo); PIN(lno);
    STAMP(1);
    LD_EPOCH(epoch);
    const u32 tag = (u32)epoch + 1u, th = (u32)((epoch + 1ull) >> 32);
    WAITV(4);
    LBAR();
    STAMP(2);
    {
      v4u A[5], xb[5];
#pragma unroll
      for (int i = 0; i < 5; ++i) {
        const v2u l2 = trr(L + tra + i * 8192), h2 = trr(L + trb + i * 8192); A[i] = v4u{l2.x, l2.y, h2.x, h2.y};
        xb[i] = *(const LAS v4u*)(L + xo + i * 64);
      }
      const f32x2 ln = *(LAS f32x2*)(L + lno);
      f32x4 acc = Z4;
#pragma unroll
      for (int i = 0; i < 5; ++i) acc = mfma32(A[i], xb[i], acc);
      f32x4 o;
#pragma unroll
      for (int i = 0; i < 4; ++i) o[i] = (acc[i] - ln.x * dpp<0x4E>(acc[i])) * ln.y;
      st_sc1(rs, qgo, v4u{pk2(o[0], o[1]), tag, pk2(o[2], o[3]), tag});
      STAMP(3);
    }
    v4u wk[4];
    int qb = QV + (lane & 7) * 272 + gq * 16;
    int qao = (r16 == 0 || r16 == 4) ? OFF_QA + (((r16 >> 2) * 32 + h) * 512 + part * 128 + wave * 16 + gq * 4) * 2 : OOB;
    PIN(qb); PIN(qao);
    WAITV(1);
#pragma unroll
    for (int s = 0; s < 4; ++s) { wk[s] = *(const LAS v4u*)(L + WK + off256(wave * 16 + r16, s * 4 + gq)); PIN(wk[s]); }
    if (wave < 4) {
      int goff = OFF_QG + h * 4096 + tid * 16, qw = QV + (((lane >> 2) & 1) * 4 + wave) * 272 + (lane >> 3) * 32 + (lane & 3) * 8;
      PIN(goff); PIN(qw);
      v4u v;
      for (int sp = 0;; ++sp) {
        CBAR();
        v = ld_sc1(rs, goff);
        const bool ok = (v.y == tag) & (v.w == tag);
        if (__all(ok) || sp > SPIN_CAP) break;
      }
      *(LAS v2u*)(L + qw) = v2u{v.x, v.z};
      STAMP(4);
    } else if (wave == 4) {
      for (int sp = 0;; ++sp) {
        CBAR();
        const v2u rv = __builtin_amdgcn_raw_buffer_load_b64(rs, OFF_READY + lane * 8, 0, 16);
        const bool ok = (rv.x == tag) & (rv.y == th);
        if (__all(ok) || sp > SPIN_CAP) break;
        __builtin_amdgcn_s_sleep(4);
      }
    }
    LBAR();
    {
      f32x4 acc = Z4;
#pragma unroll
      for (int s = 0; s < 4; ++s) acc = mfma32(wk[s], *(const LAS v4u*)(L + qb + s * 64), acc);
      f32x4 o;
#pragma unroll
      for (int i = 0; i < 4; ++i) { o[i] = acc[i] + dpp<0xB1>(acc[i]); o[i] += dpp<0x4E>(o[i]); }
      st_sc1(rs, qao, v2u{pk2(o[0], o[1]), pk2(o[2], o[3])});
      STAMP(5);
    }
    LBAR();
    const int cb = r16 & 7, ks = r16 >> 3;
    const bool mks = ks;
    int rw = (r16 < 8) ? ((wave * 2 + (gq >> 1)) * 64 + cb * 8 + (gq & 1) * 4) * 4 : 4096 + lane * 16;
    const int fL = ((gi - 5) >> 1) * 8 + gx;
    if (gi < 5) {
      const int ksl = gx >> 1, ms = (gx & 1) * 5 + gi;
      while (rt() < t_start + WO_DELAY) __builtin_amdgcn_s_sleep(2);
      const int hd = ksl * 8 + wave;
      v4u T0[8], T1[8];
      {
        v4u La[8], Lb[8];
        const rsrc_t ro = mk(p.wo, 4096 * MM * 2);
        const int off = ((hd * 128 + ks * 32 + gq * 8) * MM + ms * 64 + cb * 8) * 2;
#pragma unroll
        for (int i = 0; i < 8; ++i) La[i] = ld_nt(ro, off + i * (MM * 2));
#pragma unroll
        for (int i = 0; i < 8; ++i) Lb[i] = ld_nt(ro, off + (64 + i) * (MM * 2));
        SB();
        tr8(La, T0); tr8(Lb, T1);
#pragma unroll
        for (int i = 0; i < 8; ++i) { PIN(T0[i]); PIN(T1[i]); }
      }
      const int fcol = ((lane >> 3) & 1) * 32 + gq * 8 + (lane & 3) * 2, fb = (lane >> 2) & 1;
      const u32 xw = ksl == 0 ? *(const LAS u32*)(L + XL + (fb * MM + ms * 64 + fcol) * 2) : 0u;
      int doff = OFF_D + (hd * 4 + (r16 >> 2)) * 512 + gq * 64 + (r16 & 3) * 16;
      int rr = (fb * 64 + fcol) * 4, eo = OFF_E + (ms * 4 + ksl) * 256 + lane * 4;
      f32x2 xs = {bflo(xw), bfhi(xw)};
      PIN(doff); PIN(rw); PIN(rr); PIN(eo); PIN(xs);
      STAMP(7);
      v4u d0, d1;
      for (int sp = 0;; ++sp) {
        CBAR();
        d0 = ld_sc1(rs, doff); d1 = ld_sc1(rs, doff + 256);
        if (!__any(umax2(umax4(d0), umax4(d1)) == SENT) || sp > SPIN_CAP) break;
        __builtin_amdgcn_s_sleep(1);
      }
      STAMP(8);
      f32x4 acc[8];
#pragma unroll
      for (int i = 0; i < 8; ++i) acc[i] = mfma32(d0, T0[i], Z4);
#pragma unroll
      for (int i = 0; i < 8; ++i) acc[i] = mfma32(d1, T1[i], acc[i]);
      SB();
      *(LAS f32x4*)(L + rw) = red_rows(acc, mks);
      LBAR();
      STAMP(9);
      if (wave == 0) {
        f32x2 v[8];
#pragma unroll
        for (int k = 0; k < 8; ++k) v[k] = *(const LAS f32x2*)(L + rr + k * 512);
        const f32x2 s = (((v[0] + v[1]) + (v[2] + v[3])) + ((v[4] + v[5]) + (v[6] + v[7]))) + xs;
        __builtin_amdgcn_raw_buffer_store_b32(pk2(s.x, s.y), rs, eo, 0, 16);
      }
      STAMP(10);
    } else if (gi < 13 && fL < 27) {
      const int fs = fL * 2 + ((gi - 5) & 1);
      while (rt() < t_start + FW_DELAY) __builtin_amdgcn_s_sleep(2);
      v4u T0[8], T1[8];
      {
        v4u La[8], Lb[8];
        const u16* src = ((cb & 4) ? p.w3 : p.w1) + (size_t)(wave * 64 + ks * 32 + gq * 8) * FF + fs * 32 + (cb & 3) * 8;
#pragma unroll
        for (int i = 0; i < 8; ++i) La[i] = __builtin_nontemporal_load((const GAS v4u*)(src + i * FF));
        const u16* src2 = wave < 2 ? src + (size_t)512 * FF : p.w2 + (size_t)(fs * 32 + gq * 8) * MM + (wave < 3 ? 0 : wave - 3) * 128 + r16 * 8;
        const int st2 = wave < 2 ? FF : MM;
        if (wave != 2) {
#pragma unroll
          for (int i = 0; i < 8; ++i) Lb[i] = __builtin_nontemporal_load((const GAS v4u*)(src2 + i * st2));
        }
        SB();
        tr8(La, T0); tr8(Lb, T1);
#pragma unroll
        for (int i = 0; i < 8; ++i) { PIN(T0[i]); PIN(T1[i]); }
      }
      constexpr int CS = 16384, SS = 8192, HH = 8704;
      v4u ones = {0x3F803F80u, 0x3F803F80u, 0x3F803F80u, 0x3F803F80u};
      PIN(ones);
      {
        f32x4 a[8];
#pragma unroll
        for (int i = 0; i < 8; ++i) a[i] = mfma32(ones, T0[i], Z4);
        if (wave < 2) {
#pragma unroll
          for (int i = 0; i < 8; ++i) a[i] = mfma32(ones, T1[i], a[i]);
        }
        if (lane < 16) {
          LAS f32x4* dst = (LAS f32x4*)(L + CS + ((wave * 2 + ks) * 64 + cb * 8) * 4);
          dst[0] = f32x4{a[0][0], a[1][0], a[2][0], a[3][0]};
          dst[1] = f32x4{a[4][0], a[5][0], a[6][0], a[7][0]};
        }
      }
      LBAR();
      float cs = 0.f;
#pragma unroll
      for (int k = 0; k < 16; ++k) cs += *(const LAS float*)(L + CS + (k * 64 + lane) * 4);
      int eoff = OFF_E + (wave * 4 + (r16 >> 2)) * 256 + gq * 64 + (r16 & 3) * 16;
      int eoff2 = wave < 2 ? eoff + 8192 : OOB;
      int sw = (lane & 28) == 0 ? SS + ((lane >> 5) * 32 + (lane & 1) * 16 + wave * 2 + ((lane >> 1) & 1)) * 4 : 4096 + lane * 4;
      int rr = ((wave & 1) * 64 + lane) * 4, sr = SS + ((lane >> 5) * 32 + (wave & 1) * 16 + r16) * 4;
      int hw = lane < 32 ? HH + (wave & 1) * 64 + lane * 2 : 4096 + lane * 4, hr = HH + (r16 >> 3) * 64 + gq * 16;
      const int col = (wave - 3) * 128 + r16 * 8 + (gq & 1) * 4;
      int fo = OFF_FIN + (((gq >> 1) * 20 + (col >> 5)) * 54 + fs) * 128 + (col & 31) * 4;
      PIN(cs); PIN(eoff); PIN(eoff2); PIN(rw); PIN(sw); PIN(rr); PIN(sr); PIN(hw); PIN(hr); PIN(fo);
      STAMP(7);
      v4u e0, e1;
      for (int sp = 0;; ++sp) {
        CBAR();
        e0 = ld_sc1(rs, eoff); e1 = ld_sc1(rs, eoff2);
        if (!__any(umax2(umax4(e0), umax4(e1)) == SENT) || sp > SPIN_CAP) break;
        __builtin_amdgcn_s_sleep(1);
      }
      STAMP(11);
      f32x4 gr = mfma32(e0, e0, Z4), sm = mfma32(e0, ones, Z4);
      f32x4 acc[8];
#pragma unroll
      for (int i = 0; i < 8; ++i) acc[i] = mfma32(e0, T0[i], Z4);
      if (wave < 2) {
        gr = mfma32(e1, e1, gr); sm = mfma32(e1, ones, sm);
#pragma unroll
        for (int i = 0; i < 8; ++i) acc[i] = mfma32(e1, T1[i], acc[i]);
      }
      SB();
      {
        const bool m1 = lane & 1, m2 = lane & 2;
        float t2 = sel(m2, sel(m1, gr[3], gr[2]), sel(m1, gr[1], gr[0]));
        const float t1 = sel(m2, sel(m1, sm[3], sm[2]), sel(m1, sm[1], sm[0]));
        t2 += dpp<0x128>(t2); t2 += dpp<0x124>(t2);
        float t = red32(t1, t2);
        t = red16(t, t);
        *(LAS float*)(L + sw) = t;
      }
      *(LAS f32x4*)(L + rw) = red_rows(acc, mks);
      LBAR();
      STAMP(12);
      if (wave < 2) {
        float v[8];
        float t = *(const LAS float*)(L + sr);
#pragma unroll
        for (int k = 0; k < 8; ++k) v[k] = *(const LAS float*)(L + rr + k * 512);
        t = row_sum16(t);
        const float mean = rdl(t, 0) * (1.0f / MM);
        const float var = fmaxf(rdl(t, 32) * (1.0f / MM) - mean * mean, 0.f);
        const float rstd = __builtin_amdgcn_rsqf(var + 1e-5f);
        const float s = ((v[0] + v[1]) + (v[2] + v[3])) + ((v[4] + v[5]) + (v[6] + v[7]));
        const float y = (s - mean * cs) * rstd;
        auto sw2 = __builtin_amdgcn_permlane32_swap(__float_as_uint(y), __float_as_uint(y), false, false);
        const float gt = __uint_as_float(sw2[0]), up = __uint_as_float(sw2[1]);
        const float hh = gt * up * __builtin_amdgcn_rcpf(1.0f + __builtin_amdgcn_exp2f(-1.4426950408889634f * gt));
        *(LAS u16*)(L + hw) = (u16)pk2(hh, 0.f);
      }
      LBAR();
      STAMP(13);
      if (wave >= 3) {
        const v4u A = *(const LAS v4u*)(L + hr);
#pragma unroll
        for (int i = 0; i < 8; ++i) acc[i] = mfma32(A, T1[i], Z4);
        SB();
          const bool mh = gq & 1;
        st_sc1(rs, fo, v4u{__float_as_uint(sel(mh, acc[4][0], acc[0][0])), __float_as_uint(sel(mh, acc[5][0], acc[1][0])),
                           __float_as_uint(sel(mh, acc[6][0], acc[2][0])), __float_as_uint(sel(mh, acc[7][0], acc[3][0]))});
        STAMP(14);
      }
    }
  } else {
    const int a = bid - 128, b = a >> 6;
    const int vg = (a & 7) * 2 + (a >> 6), vj = vg & 3, vh = (vg >> 2) * 8 + ((a >> 3) & 7), vi = vh * 4 + vj;
    const int r32 = lane & 31, hf = lane >> 5;
    constexpr int QA = 131072, TM = 32768, LS = 33280, PB = 34816, OT = 49152, WB = 98304, ML = 98560, R1 = 98816, RFN = 100352;
    v4u qv[4];
    const u32 qoff = OFF_QA + b * 32768 + tid * 16;
#define LDQ(i) asm volatile("global_load_dwordx4 %0, %1, %2 sc1" : "=v"(qv[i]) : "v"(qoff + (i) * 8192), "s"(ws) : "memory")
#define STAGE_Q() \
    asm volatile("s_waitcnt vmcnt(0)" : "+v"(qv[0]), "+v"(qv[1]), "+v"(qv[2]), "+v"(qv[3]) :: "memory"); \
    for (int sp = 0;; ++sp) { \
      u32 mx = 0; \
      _Pragma("unroll") for (int i = 0; i < 4; ++i) { mx = umax2(mx, umax4(qv[i])); *(LAS v4u*)(L + qs[i & 1] + (i >> 1) * 16384) = qv[i]; } \
      if (!__any(mx == SENT) || sp > SPIN_CAP) break; \
      LDQ(0); LDQ(1); LDQ(2); LDQ(3); \
      asm volatile("s_waitcnt vmcnt(0)" : "+v"(qv[0]), "+v"(qv[1]), "+v"(qv[2]), "+v"(qv[3]) :: "memory"); \
    }
    int qs[2] = {QA + kvoff(wave, lane), QA + kvoff(8 + wave, lane)};
    u32 tag, th;
    if (wave < 4) {
      while (rt() < t_start + ATT_DELAY) __builtin_amdgcn_s_sleep(1);
      BAR();
      const u32 kvo = (u32)(b * 8192 + wave * 64) * 1024u;
#define KVD(t, i) dma16<true>(p.ckv, kvo + (t) * 2097152u + (i) * 262144u + ((a + KV_ROT * ((i) + 2 * wave)) & 63) * 1024u + ((lane ^ ((((i) & 3) << 2) | ((2 * wave + ((i) >> 2)) & 3))) * 16), ((t) * 32 + wave * 8 + (i)) * 1024)
#define KVDMA(t) KVD(t, 0); KVD(t, 1); KVD(t, 2); KVD(t, 3); KVD(t, 4); KVD(t, 5); KVD(t, 6); KVD(t, 7)
      v2u rv;
      const u32 ro = OFF_READY + lane * 8;
      KVDMA(0); KVDMA(1);
      asm volatile("s_cmp_lg_u32 %3, 0\n\ts_cbranch_scc1 1f\n\tglobal_load_dwordx2 %0, %1, %2 sc1\n1:" : "=v"(rv) : "v"(ro), "s"(ws), "s"(wave) : "memory", "scc");
      KVDMA(2);
      WAITV(16); BAR();
      STAMP(1);
      KVDMA(3);
      LD_EPOCH(epoch);
      tag = (u32)epoch + 1u; th = (u32)((epoch + 1ull) >> 32);
      asm volatile("s_waitcnt vmcnt(16)" : "+v"(rv) :: "memory");
      if (wave == 0) for (int sp = 0;; ++sp) {
        const bool ok = (rv.x == tag) & (rv.y == th);
        if (__all(ok) || sp > SPIN_CAP) break;
        asm volatile("global_load_dwordx2 %0, %1, %2 sc1\n\ts_waitcnt vmcnt(0)" : "=&v"(rv) : "v"(ro), "s"(ws) : "memory");
      }
      BAR();
      LDQ(0); LDQ(1); LDQ(2); LDQ(3);
      int ab[8];
#pragma unroll
      for (int k = 0; k < 8; ++k) { ab[k] = kvoff(r32, 2 * k + hf); PIN(ab[k]); }
      PIN(qs[0]); PIN(qs[1]);
      v4u fr[32];
      const int tb = wave * 32768;
#define AFRAGS() _Pragma("unroll") for (int s = 0; s < 32; ++s) fr[s] = *(const LAS v4u*)(L + tb + ab[s & 7] + (s >> 3) * 256)
      if (wave < 2) { AFRAGS(); }
      WAITV(12); LBAR();
      STAMP(17);
      if (wave == 2) { AFRAGS(); }
      if (wave == 0 && lane < 32) *(LAS v4u*)(L + TM + lane * 16) = SV;
      STAGE_Q();
      STAMP(4);
      LBAR();
      STAMP(5);
      f32x16 s;
#pragma unroll
      for (int i = 0; i < 16; ++i) s[i] = 0.f;
      v4u bq[8];
      if (wave == 3) {
#pragma unroll
        for (int k = 0; k < 6; ++k) { bq[k] = *(const LAS v4u*)(L + QA + ab[k]); fr[k] = *(const LAS v4u*)(L + 98304 + ab[k]); }
        SB();
#pragma unroll
        for (int k = 0; k < 32; ++k) {
          s = mfmaB(fr[k % 6], bq[k % 6], s);
          if (k + 6 < 32) { bq[k % 6] = *(const LAS v4u*)(L + QA + ab[(k + 6) & 7] + ((k + 6) >> 3) * 256); fr[k % 6] = *(const LAS v4u*)(L + 98304 + ab[(k + 6) & 7] + ((k + 6) >> 3) * 256); }
          SB();
        }
      } else {
#pragma unroll
        for (int k = 0; k < 8; ++k) bq[k] = *(const LAS v4u*)(L + QA + ab[k]);
        SB();
#pragma unroll
        for (int k = 0; k < 32; ++k) {
          s = mfmaB(fr[k], bq[k & 7], s);
          if (k + 8 < 32) bq[k & 7] = *(const LAS v4u*)(L + QA + ab[k & 7] + ((k + 8) >> 3) * 256);
          SB();
        }
      }
      float m = fmaxf(fmaxf(fmaxf(fmaxf(s[0], s[1]), s[2]), fmaxf(fmaxf(s[3], s[4]), s[5])), fmaxf(fmaxf(fmaxf(s[6], s[7]), s[8]), fmaxf(fmaxf(s[9], s[10]), s[11])));
      m = fmaxf(m, fmaxf(fmaxf(fmaxf(s[12], s[13]), s[14]), s[15]));
      m = max32(m);
      *(volatile LAS float*)(L + TM + r32 * 16 + wave * 4) = m;
      STAMP(6);
      v4u tm;
      for (int sp = 0;; ++sp) {
        tm = *(volatile LAS v4u*)(L + TM + r32 * 16);
        if (!__any(umax4(tm) == SENT) || sp > SPIN_CAP) break;
      }
      m = fmaxf(fmaxf(__uint_as_float(tm.x), __uint_as_float(tm.y)), fmaxf(__uint_as_float(tm.z), __uint_as_float(tm.w)));
#pragma unroll
      for (int i = 0; i < 16; ++i) s[i] = __builtin_amdgcn_exp2f(s[i] - m);
      *(LAS v4u*)(L + PB + wave * 2048 + lane * 16) = v4u{pk2(s[0], s[1]), pk2(s[2], s[3]), pk2(s[4], s[5]), pk2(s[6], s[7])};
      *(LAS v4u*)(L + PB + wave * 2048 + 1024 + lane * 16) = v4u{pk2(s[8], s[9]), pk2(s[10], s[11]), pk2(s[12], s[13]), pk2(s[14], s[15])};
      LBAR();
      float l = 0.f;
#pragma unroll
      for (int i = 0; i < 16; ++i) l += s[i];
      *(LAS float*)(L + LS + (r32 * 8 + wave * 2 + hf) * 4) = l;
      LBAR();
      int soff = OFF_ST + (vj * 128 + wave * 32 + r32) * 512 + vh * 16;
      int ioff = OFF_IN + vi * 32768 + (wave * 32 + gq * 8) * 256 + r16 * 16;
      int r1w = lane < 16 ? R1 + wave * 272 + r16 * 16 : OT + lane * 16;
      PIN(ioff); PIN(soff);
      BAR();
      v4u sv;
      for (int sp = 0;; ++sp) {
        CBAR();
        sv = ld_sc1(rs, soff);
        const bool ok = (sv.y == tag) & (sv.w == tag);
        if (__all(ok) || sp > SPIN_CAP) break;
        __builtin_amdgcn_s_sleep(1);
      }
      if (C_WAIT) __builtin_amdgcn_s_sleep(C_WAIT);
      v4u pv[8];
      CBAR();
#pragma unroll
      for (int i = 0; i < 8; ++i) pv[i] = ld_sc1(rs, ioff + i * 256);
      SB();
      STAMP(10);
      const float ms = __uint_as_float(sv.x);
      float mw = fmaxf(ms, dpp<0xB1>(ms)); mw = fmaxf(mw, dpp<0x4E>(mw)); mw = fmaxf(mw, dpp<0x141>(mw)); mw = fmaxf(mw, dpp<0x140>(mw));
      mw = max16(mw);
      const u32 wq16 = pk2(__builtin_amdgcn_exp2f(ms - mw), 0.f);
      float lw = row_sum16(bflo(wq16) * __uint_as_float(sv.z));
      lw = red16(lw, lw);
      *(LAS u16*)(L + WB + wave * 64 + r32 * 2) = (u16)wq16;
      *(LAS f32x2*)(L + ML + wave * 8) = f32x2{mw, lw};
      v4u Aw = *(const LAS v4u*)(L + WB + wave * 64 + gq * 16);
      PIN(Aw); PIN(r1w);
      for (int sp = 0;; ++sp) {
        u32 mx = 0;
#pragma unroll
        for (int i = 0; i < 8; ++i) mx = umax2(mx, umax4(pv[i]));
        if (!__any(mx == SENT) || sp > SPIN_CAP) break;
        CBAR();
#pragma unroll
        for (int i = 0; i < 8; ++i) pv[i] = ld_sc1(rs, ioff + i * 256);
      }
      STAMP(11);
      v4u T[8];
      tr8(pv, T);
      f32x4 c[8];
#pragma unroll
      for (int i = 0; i < 8; ++i) c[i] = mfma32(Aw, T[i], Z4);
      *(LAS v4u*)(L + r1w) = v4u{pk2(c[0][0], c[1][0]), pk2(c[2][0], c[3][0]), pk2(c[4][0], c[5][0]), pk2(c[6][0], c[7][0])};
      LBAR();
    } else {
      const int u = wave - 4;
      if (wave == 7 && a < 64) {
        st_sc1(rs, OFF_D + a * 1024 + lane * 16, SV);
        st_sc1(rs, OFF_QA + a * 1024 + lane * 16, SV);
        st_sc1(rs, a < 10 ? OFF_E + a * 1024 + lane * 16 : OOB, SV);
        LD_EPOCH(epoch);
        WAITV(0);
        st_sc1(rs, lane == 0 ? OFF_READY + a * 8 : OOB, v2u{(u32)epoch + 1u, (u32)((epoch + 1ull) >> 32)});
      } else {
        if (a >= 88) {
#pragma unroll
          for (int i = 0; i < 2; ++i) { const int o = (u * 2 + i) * 1024 + lane * 16; st_sc1(rs, o < 54 * 128 ? OFF_FIN + (a - 88) * (54 * 128) + o : OOB, SV); }
        }
        LD_EPOCH(epoch);
      }
      BAR();
      STAMP(16);
      tag = (u32)epoch + 1u; th = (u32)((epoch + 1ull) >> 32);
      int ab[8];
      {
        const int rg = ((lane >> 4) & 1) * 4 + (lane & 3), q = (lane >> 2) & 3;
        const int cbase = (rg & 1) * 16 + (rg >> 1) * 4;
#pragma unroll
        for (int t = 0; t < 4; ++t) {
          const int c = u * 128 + t * 32 + cbase;
          ab[2 * t] = kvoff(4 * hf + q, c >> 3) + (c & 4) * 2;
          ab[2 * t + 1] = kvoff(4 * hf + q + 8, c >> 3) + (c & 4) * 2;
        }
      }
      int ow = OT + u * 8704 + r32 * 272 + hf * 32, orr = OT + u * 8704 + (lane >> 3) * 272 + (lane & 7) * 16;
      int so = OFF_IN + ((lane >> 3) * 4 + u) * 32768 + a * 256 + (lane & 7) * 16;
      int sto = lane < 32 ? OFF_ST + (u * 128 + a) * 512 + lane * 16 : OOB;
      u32 wvo = (u32)(vj * 128 + u * 32 + gq) * 8192u + vh * 256;
#pragma unroll
      for (int k = 0; k < 8; ++k) PIN(ab[k]);
      PIN(ow); PIN(orr); PIN(so); PIN(sto); PIN(wvo); PIN(qs[0]); PIN(qs[1]);
      v4u fr[32];
#define VFRAGS(t) \
      _Pragma("unroll") for (int tt = 0; tt < 4; ++tt) _Pragma("unroll") for (int k2 = 0; k2 < 2; ++k2) { \
        const v2u l2 = trr(L + (t) * 32768 + k2 * 16384 + ab[2 * tt]), h2 = trr(L + (t) * 32768 + k2 * 16384 + ab[2 * tt + 1]); \
        fr[tt * 8 + (t) * 2 + k2] = v4u{l2.x, l2.y, h2.x, h2.y}; }
      BAR(); VFRAGS(0);
      STAMP(1);
      BAR();
      LDQ(0); LDQ(1); LDQ(2); LDQ(3);
      VFRAGS(1);
      LBAR(); VFRAGS(2);
      STAMP(17);
      STAGE_Q();
      STAMP(4);
      LBAR();
      STAMP(5);
#pragma unroll
      for (int i = 0; i < 4; ++i) { st_sc1(rs, so + i * (32 * 32768), SV); st_sc1(rs, so + 128 + i * (32 * 32768), SV); }
      VFRAGS(3);
      SB();
#pragma unroll
      for (int i = 0; i < 8; ++i) dma16<true>(p.wuv, wvo + i * 32768 + ((r16 ^ ((gq << 2) | (i & 3))) * 16), (u * 8 + i) * 1024);
      STAMP(6);
      LBAR();
      STAMP(7);
      v4u pf[8];
#pragma unroll
      for (int k = 0; k < 8; ++k) pf[k] = *(const LAS v4u*)(L + PB + k * 1024 + lane * 16);
      f32x16 o0, o1, o2, o3;
#pragma unroll
      for (int i = 0; i < 16; ++i) { o0[i] = 0.f; o1[i] = 0.f; o2[i] = 0.f; o3[i] = 0.f; }
#define PK8(o, q) v4u{pk2(o[8 * (q)], o[8 * (q) + 1]), pk2(o[8 * (q) + 2], o[8 * (q) + 3]), pk2(o[8 * (q) + 4], o[8 * (q) + 5]), pk2(o[8 * (q) + 6], o[8 * (q) + 7])}
#pragma unroll
      for (int k = 0; k < 8; ++k) {
        o0 = mfmaB(fr[k], pf[k], o0); o1 = mfmaB(fr[8 + k], pf[k], o1);
        if (k == 3) { SB(); BAR(); SB(); }
      }
      SB();
      const f32x4 tm = *(LAS f32x4*)(L + TM + r32 * 16), l0 = *(LAS f32x4*)(L + LS + r32 * 32), l1 = *(LAS f32x4*)(L + LS + r32 * 32 + 16);
      v4u tv[4];
#pragma unroll
      for (int k = 0; k < 8; ++k) {
        o2 = mfmaB(fr[16 + k], pf[k], o2);
        o3 = mfmaB(fr[24 + k], pf[k], o3);
        if (k == 1) { *(LAS v4u*)(L + ow) = PK8(o0, 0); *(LAS v4u*)(L + ow + 16) = PK8(o0, 1); }
        if (k == 2) { *(LAS v4u*)(L + ow + 64) = PK8(o1, 0); *(LAS v4u*)(L + ow + 80) = PK8(o1, 1); }
        if (k == 3) { _Pragma("unroll") for (int i = 0; i < 4; ++i) tv[i] = *(const LAS v4u*)(L + orr + i * 2176); }
        if (k == 4) WAITV(8);
        if (k == ST_K) {
          st_sc1(rs, sto, v4u{__float_as_uint(fmaxf(fmaxf(tm.x, tm.y), fmaxf(tm.z, tm.w))), tag, __float_as_uint(sum4(l0) + sum4(l1)), tag});
        }
        if (k == 5) { _Pragma("unroll") for (int i = 0; i < 4; ++i) st_sc1(rs, so + i * (32 * 32768), tv[i]); }
        SB();
      }
      STAMP(8);
      *(LAS v4u*)(L + ow + 128) = PK8(o2, 0); *(LAS v4u*)(L + ow + 144) = PK8(o2, 1);
      *(LAS v4u*)(L + ow + 192) = PK8(o3, 0); *(LAS v4u*)(L + ow + 208) = PK8(o3, 1);
#pragma unroll
      for (int i = 0; i < 4; ++i) tv[i] = *(const LAS v4u*)(L + orr + 128 + i * 2176);
#pragma unroll
      for (int i = 0; i < 4; ++i) st_sc1(rs, so + 128 + i * (32 * 32768), tv[i]);
      STAMP(18);
      WAITV(9);
      BAR();
      v4u Av[8];
      {
        const int q4 = (lane >> 2) & 3, pp = lane & 3;
#pragma unroll
        for (int t = 0; t < 2; ++t)
#pragma unroll
          for (int s = 0; s < 4; ++s) {
            const int row = s * 32 + gq * 8 + q4, ch = (u >> 1) * 8 + t * 4 + (u & 1) * 2 + (pp >> 1);
            const v2u l2 = trr(L + off256(row, ch) + (pp & 1) * 8), h2 = trr(L + off256(row + 4, ch) + (pp & 1) * 8);
            Av[t * 4 + s] = v4u{l2.x, l2.y, h2.x, h2.y};
          }
      }
#pragma unroll
      for (int s = 0; s < 8; ++s) PIN(Av[s]);
      int dso = r16 < 4 ? OFF_D + vi * 512 + u * 128 + (gq >> 1) * 64 + (r16 & 1) * 32 + (r16 >> 1) * 16 + (gq & 1) * 8 : OOB;
      int rb = R1 + (lane & 3) * 272 + gq * 16, mlo = ML + (lane & 2) * 8;
      PIN(dso); PIN(rb); PIN(mlo);
      STAMP(9);
      LBAR();
      const f32x4 ml = *(const LAS f32x4*)(L + mlo);
      v4u bb[4];
#pragma unroll
      for (int s = 0; s < 4; ++s) bb[s] = *(const LAS v4u*)(L + rb + s * 64);
      f32x4 a0 = Z4, a1 = Z4;
#pragma unroll
      for (int s = 0; s < 4; ++s) { a0 = mfma32(Av[s], bb[s], a0); a1 = mfma32(Av[4 + s], bb[s], a1); }
      const bool odd = lane & 1;
      const float M = fmaxf(ml.x, ml.z), e0 = __builtin_amdgcn_exp2f(ml.x - M), e1 = __builtin_amdgcn_exp2f(ml.z - M);
      const float sc = (odd ? e1 : e0) * __builtin_amdgcn_rcpf(e0 * ml.y + e1 * ml.w);
      f32x4 o;
#pragma unroll
      for (int i = 0; i < 4; ++i) o[i] = (odd ? a1[i] : a0[i]) * sc + dpp<0xB1>((odd ? a0[i] : a1[i]) * sc);
      st_sc1(rs, dso, v2u{pk2(o[0], o[1]), pk2(o[2], o[3])});
      STAMP(12);
    }
    if (a >= 88) {
      const int red = a - 88, bb = red / 20, c0 = (red % 20) * 32;
      float h0 = 0.f, h1 = 0.f;
      if (wave == 7) {
        u32 ew;
        const int eoff = OFF_E + ((c0 >> 6) * 4 + gq) * 256 + (r16 >> 2) * 64 + ((c0 >> 5) & 1) * 32 + bb * 16 + (r16 & 3) * 4;
        for (int sp = 0;; ++sp) {
          CBAR();
          ew = __builtin_amdgcn_raw_buffer_load_b32(rs, eoff, 0, 16);
          if (!__any(ew == SENT) || sp > SPIN_CAP) break;
          __builtin_amdgcn_s_sleep(4);
        }
        h0 = bflo(ew); h1 = bfhi(ew);
        h0 = red16(h0, h0); h1 = red16(h1, h1); h0 = red32(h0, h0); h1 = red32(h1, h1);
      }
      int foff = tid < 54 * 8 ? OFF_FIN + red * (54 * 128) + tid * 16 : OOB;
      int fw = r16 < 8 ? RFN + (wave * 32 + r16 * 4 + gq) * 4 : OT + lane * 4, frd = RFN + r16 * 8;
      PIN(h0); PIN(h1); PIN(foff); PIN(fw); PIN(frd);
      BAR();
      STAMP(13);
      if (wave < 7) {
        v4u f0;
        for (int sp = 0;; ++sp) {
          CBAR();
          f0 = ld_sc1(rs, foff);
          if (!__any(umax4(f0) == SENT) || sp > SPIN_CAP) break;
          __builtin_amdgcn_s_sleep(1);
        }
        STAMP(14);
        const f32x4 s = __builtin_bit_cast(f32x4, f0);
        const float b0 = red32(s[0], s[2]), b1 = red32(s[1], s[3]);
        float c = red16(b0, b1);
        c += dpp<0x128>(c);
        *(LAS float*)(L + fw) = c;
      }
      LBAR();
      if (wave == 7) {
        f32x2 v[7];
#pragma unroll
        for (int w = 0; w < 7; ++w) v[w] = *(LAS f32x2*)(L + frd + w * 128);
        const f32x2 s = ((v[0] + v[1]) + (v[2] + v[3])) + ((v[4] + v[5]) + (v[6] + f32x2{h0, h1}));
        if (lane < 16) ((GAS u32*)(p.out + bb * MM + c0))[lane] = pk2(s.x, s.y);
        if (red == 0) st_sc1(rs, lane == 0 ? OFF_EPOCH : OOB, v2u{tag, th});
      }
      STAMP(15);
    }
  }
  STAMP_FLUSH();
}

extern "C" void kernel_launch(void* const* d_in, const int* in_sizes, int n_in, void* d_out, int out_size, void* d_ws, size_t ws_size,
                              hipStream_t stream) {
  Params p;
  p.x = (const u16*)d_in[0]; p.ckv = (const u16*)d_in[1]; p.wq = (const u16*)d_in[2]; p.wuk = (const u16*)d_in[3];
  p.wuv = (const u16*)d_in[4]; p.wo = (const u16*)d_in[5]; p.w1 = (const u16*)d_in[7]; p.w3 = (const u16*)d_in[8]; p.w2 = (const u16*)d_in[9];
  p.out = (u16*)d_out; p.ws = (unsigned char*)d_ws;
  (void)hipFuncSetAttribute((const void*)mega, hipFuncAttributeMaxDynamicSharedMemorySize, 163840);
  mega<<<256, 512, 163840, stream>>>(p);
}
```
